```python
import torch

def _softmax(x, axis=-1):
    return torch.softmax(x, dim=axis)

def _gelu(x):
    return torch.nn.functional.gelu(x, approximate='tanh')

def _silu(x):
    return torch.nn.functional.silu(x)

def _sigmoid(x):
    return torch.sigmoid(x)

def _layernorm(x, axis=-1, eps=1e-05):
    m = x.mean(dim=axis, keepdim=True)
    v = x.var(dim=axis, keepdim=True, unbiased=False)
    return (x - m) / torch.sqrt(v + eps)

def _scatter_add(tgt, idx, src, axis=0):
    return tgt.clone().index_add_(axis, idx, src)

def _segment_sum(src, seg, n):
    out = torch.zeros((n,) + src.shape[1:], dtype=src.dtype)
    return out.index_add_(0, seg, src)

def _pad(x, pw):
    flat = tuple((v for pair in reversed(pw) for v in pair))
    return torch.nn.functional.pad(x, flat)
kernel_name = 'adaLN_cond__compressed_kv_cla__glu_gated_glu__decode'

def setup_inputs():

    _w = lambda a: (lambda t: t.to(torch.bfloat16) if t.is_floating_point() else t)(torch.as_tensor(a))

    def _A_setup():
        B, H, Sq, Sk, D, M = (2, 32, 1, 4096, 128, 640)
        Dc = 4 * D
        return {'x': _w((lambda *s: torch.randn(*s, dtype=torch.bfloat16) * 0.05)(B, Sq, M)), 'c_kv': _w((lambda *s: torch.randn(*s, dtype=torch.bfloat16) * 1.6)(B, Sk, Dc)), 'w_q': _w((lambda *s: torch.randn(*s, dtype=torch.bfloat16) * 0.8)(M, H, D) * M ** (-0.5)), 'w_uk': _w((lambda *s: torch.randn(*s, dtype=torch.bfloat16) * 0.8)(Dc, H, D) * Dc ** (-0.5)), 'w_uv': _w((lambda *s: torch.randn(*s, dtype=torch.bfloat16) * 1.6)(Dc, H, D) * Dc ** (-0.5)), 'w_o': _w((lambda *s: torch.randn(*s, dtype=torch.bfloat16) * 1.6)(H, D, M) * (H * D) ** (-0.5))}
    _A_in = _A_setup()

    def _B_setup():
        B, S, D, F = (2, 1, 640, 1728)
        return {'x': _w((lambda *s: torch.randn(*s, dtype=torch.bfloat16) * 0.05)(B, S, D)), 'w_gate': _w((lambda *s: torch.randn(*s, dtype=torch.bfloat16) * 0.05)(D, F)), 'w_up': _w((lambda *s: torch.randn(*s, dtype=torch.bfloat16) * 0.05)(D, F)), 'w_down': _w((lambda *s: torch.randn(*s, dtype=torch.bfloat16) * 0.05)(F, D))}
    _B_in = _B_setup()
    return {'A_x': _A_in['x'], 'A_c_kv': _A_in['c_kv'], 'A_w_q': _A_in['w_q'], 'A_w_uk': _A_in['w_uk'], 'A_w_uv': _A_in['w_uv'], 'A_w_o': _A_in['w_o'], 'B_x': _B_in['x'], 'B_w_gate': _B_in['w_gate'], 'B_w_up': _B_in['w_up'], 'B_w_down': _B_in['w_down'], 'c': _w((lambda *s: torch.randn(*s, dtype=torch.bfloat16) * 0.1)(2, 640) * 8.0), 'w_mod': _w((lambda *s: torch.randn(*s, dtype=torch.bfloat16) * 0.1)(640, 6 * 640) * 640 ** (-0.5) * 8.0)}

def reference(A_x, A_c_kv, A_w_q, A_w_uk, A_w_uv, A_w_o, B_x, B_w_gate, B_w_up, B_w_down, c, w_mod):

    def _A_ref(x, c_kv, w_q, w_uk, w_uv, w_o):
        B, Sq, M = (x.shape[0], x.shape[1], x.shape[2])
        Sk = c_kv.shape[1]
        H, D = (w_q.shape[1], w_q.shape[2])
        q = torch.einsum('bqm,mhd->bhqd', x, w_q)
        k = torch.einsum('bkc,chd->bhkd', c_kv, w_uk)
        v = torch.einsum('bkc,chd->bhkd', c_kv, w_uv)
        scores = torch.einsum('bhqd,bhkd->bhqk', q, k) * D ** (-0.5)
        off = Sk - Sq
        qi = torch.arange(Sq)[:, None]
        kj = torch.arange(Sk)[None, :]
        cmask = kj <= qi + off
        scores = torch.where(cmask[None, None], scores, -1000000000.0)
        p = _softmax(scores, axis=-1)
        ctx = torch.einsum('bhqk,bhkd->bhqd', p, v)
        return torch.einsum('bhqd,hdm->bqm', ctx, w_o)

    def _B_ref(x, w_gate, w_up, w_down):
        g = torch.einsum('bsd,df->bsf', x, w_gate)
        u = torch.einsum('bsd,df->bsf', x, w_up)
        h = _sigmoid(g) * u
        return torch.einsum('bsf,fd->bsd', h, w_down)
    x = A_x
    mod = (_silu(c) @ w_mod).reshape(c.shape[0], 6, 1, -1)
    sa, ca, ga, sb, cb, gb = (mod[:, 0].clone(), mod[:, 1].clone(), mod[:, 2].clone(), mod[:, 3].clone(), mod[:, 4].clone(), mod[:, 5].clone())
    h = _layernorm(x, axis=-1) * (1 + ca) + sa
    x = x + ga * _A_ref(h, A_c_kv, A_w_q, A_w_uk, A_w_uv, A_w_o)
    h = _layernorm(x, axis=-1) * (1 + cb) + sb
    x = x + gb * _B_ref(h, B_w_gate, B_w_up, B_w_down)
    return x
```

```cpp
#include <hip/hip_runtime.h>
#include <hip/hip_bf16.h>
#include <stdint.h>

typedef unsigned short u16;
__device__ __forceinline__ float bf2f(u16 v) { return __uint_as_float(((unsigned)v) << 16); }
__device__ __forceinline__ u16 f2bf(float f) { unsigned u = __float_as_uint(f); u += 0x7FFFu + ((u >> 16) & 1u); return (u16)(u >> 16); }
__device__ __forceinline__ float rbf(float f) { return bf2f(f2bf(f)); }

constexpr int B = 2, H = 32, SK = 4096, D = 128, M = 640, DC = 512, F = 1728;

__global__ void k_mod(const u16* c, const u16* wmod, float* mod) {
  __shared__ float s[2][M];
  __shared__ float red[4][2][64];
  for (int i = threadIdx.x; i < 2 * M; i += 256) { float v = bf2f(c[i]); s[i / M][i % M] = rbf(v / (1.f + __expf(-v))); }
  __syncthreads();
  int col = blockIdx.x * 64 + (threadIdx.x & 63), g = threadIdx.x >> 6;
  float a0 = 0, a1 = 0;
  for (int k = g; k < M; k += 4) { float w = bf2f(wmod[(size_t)k * 3840 + col]); a0 += s[0][k] * w; a1 += s[1][k] * w; }
  red[g][0][threadIdx.x & 63] = a0; red[g][1][threadIdx.x & 63] = a1;
  __syncthreads();
  if (threadIdx.x < 128) { int b = threadIdx.x >> 6, l = threadIdx.x & 63; float v = red[0][b][l] + red[1][b][l] + red[2][b][l] + red[3][b][l]; mod[b * 3840 + blockIdx.x * 64 + l] = rbf(v); }
}

__device__ void ln_mod(const float* xin  , const float* mod, int so, int co, float (*h)[M], float* tmp) {
  for (int b = 0; b < 2; ++b) {
    float s = 0; for (int i = threadIdx.x; i < M; i += 256) s += xin[b * M + i];
    for (int o = 32; o; o >>= 1) s += __shfl_xor(s, o);
    if ((threadIdx.x & 63) == 0) tmp[threadIdx.x >> 6] = s;
    __syncthreads();
    float mean = (tmp[0] + tmp[1] + tmp[2] + tmp[3]) / M;
    __syncthreads();
    float v = 0; for (int i = threadIdx.x; i < M; i += 256) { float d = xin[b * M + i] - mean; v += d * d; }
    for (int o = 32; o; o >>= 1) v += __shfl_xor(v, o);
    if ((threadIdx.x & 63) == 0) tmp[threadIdx.x >> 6] = v;
    __syncthreads();
    float var = (tmp[0] + tmp[1] + tmp[2] + tmp[3]) / M;
    __syncthreads();
    float rs = 1.f / sqrtf(var + 1e-5f);
    for (int i = threadIdx.x; i < M; i += 256) h[b][i] = (xin[b * M + i] - mean) * rs * (1.f + mod[b * 3840 + co + i]) + mod[b * 3840 + so + i];
  }
  __syncthreads();
}

__global__ void k_q(const u16* x, const float* mod, const u16* wq, float* q) {
  __shared__ float xs[2 * M]; __shared__ float h[2][M]; __shared__ float tmp[4]; __shared__ float red[4][2][64];
  for (int i = threadIdx.x; i < 2 * M; i += 256) xs[i] = bf2f(x[i]);
  __syncthreads();
  ln_mod(xs, mod, 0, 640, h, tmp);
  int col = blockIdx.x * 64 + (threadIdx.x & 63), g = threadIdx.x >> 6;
  float a0 = 0, a1 = 0;
  for (int k = g; k < M; k += 4) { float w = bf2f(wq[(size_t)k * 4096 + col]); a0 += h[0][k] * w; a1 += h[1][k] * w; }
  red[g][0][threadIdx.x & 63] = a0; red[g][1][threadIdx.x & 63] = a1;
  __syncthreads();
  if (threadIdx.x < 128) { int b = threadIdx.x >> 6, l = threadIdx.x & 63; float v = red[0][b][l] + red[1][b][l] + red[2][b][l] + red[3][b][l]; q[b * 4096 + blockIdx.x * 64 + l] = v; }
}

__global__ void k_qabs(const float* q, const u16* wuk, float* qa) {
  int h = blockIdx.x >> 3, cg = blockIdx.x & 7, w = threadIdx.x >> 6, l = threadIdx.x & 63;
  float q00 = q[h * 128 + 2 * l], q01 = q[h * 128 + 2 * l + 1], q10 = q[4096 + h * 128 + 2 * l], q11 = q[4096 + h * 128 + 2 * l + 1];
  for (int i = w; i < 64; i += 4) {
    int c = cg * 64 + i;
    unsigned ww = *(const unsigned*)(wuk + (size_t)c * 4096 + h * 128 + 2 * l);
    float w0 = __uint_as_float(ww << 16), w1 = __uint_as_float(ww & 0xffff0000u);
    float a0 = q00 * w0 + q01 * w1, a1 = q10 * w0 + q11 * w1;
    for (int o = 32; o; o >>= 1) { a0 += __shfl_xor(a0, o); a1 += __shfl_xor(a1, o); }
    if (l == 0) { qa[(0 * H + h) * DC + c] = a0 * 0.08838834764831845f; qa[(1 * H + h) * DC + c] = a1 * 0.08838834764831845f; }
  }
}

__global__ void k_attn(const float* qa, const u16* ckv, float* po, float* pm, float* pl) {
  __shared__ float ck[32][DC + 4];
  __shared__ float s[H][32];
  int b = blockIdx.x >> 7, ch = blockIdx.x & 127;
  const u16* src = ckv + ((size_t)b * SK + ch * 32) * DC;
  for (int i = threadIdx.x; i < 32 * DC; i += 256) ck[i / DC][i % DC] = bf2f(src[i]);
  __syncthreads();
  for (int i = threadIdx.x; i < H * 32; i += 256) {
    int h = i >> 5, k = i & 31; const float* qq = qa + (b * H + h) * DC; float a = 0;
    for (int c = 0; c < DC; ++c) a += qq[c] * ck[k][c];
    s[h][k] = a;
  }
  __syncthreads();
  if (threadIdx.x < H) {
    int h = threadIdx.x; float m = -1e30f; for (int k = 0; k < 32; ++k) m = fmaxf(m, s[h][k]);
    float l = 0; for (int k = 0; k < 32; ++k) { float p = __expf(s[h][k] - m); s[h][k] = p; l += p; }
    pm[(b * H + h) * 128 + ch] = m; pl[(b * H + h) * 128 + ch] = l;
  }
  __syncthreads();
  for (int i = threadIdx.x; i < H * DC; i += 256) {
    int h = i / DC, c = i % DC; float a = 0;
    for (int k = 0; k < 32; ++k) a += s[h][k] * ck[k][c];
    po[((size_t)(b * H + h) * 128 + ch) * DC + c] = a;
  }
}

__global__ void k_ctx(const float* po, const float* pm, const float* pl, const u16* wuv, float* ctx) {
  __shared__ float w[128]; __shared__ float cc[DC]; __shared__ float red[2][128];
  int bh = blockIdx.x, h = bh & 31;
  if (threadIdx.x == 0) {
    float m = -1e30f; for (int i = 0; i < 128; ++i) m = fmaxf(m, pm[bh * 128 + i]);
    float l = 0; for (int i = 0; i < 128; ++i) { float e = __expf(pm[bh * 128 + i] - m); w[i] = e; l += e * pl[bh * 128 + i]; }
    for (int i = 0; i < 128; ++i) w[i] /= l;
  }
  __syncthreads();
  for (int c = threadIdx.x; c < DC; c += 256) { float a = 0; for (int i = 0; i < 128; ++i) a += w[i] * po[((size_t)bh * 128 + i) * DC + c]; cc[c] = a; }
  __syncthreads();
  int d = threadIdx.x & 127, g = threadIdx.x >> 7; float a = 0;
  for (int c = g; c < DC; c += 2) a += cc[c] * bf2f(wuv[(size_t)c * 4096 + h * 128 + d]);
  red[g][d] = a; __syncthreads();
  if (threadIdx.x < 128) ctx[bh * 128 + d] = red[0][d] + red[1][d];
}

__global__ void k_wo(const float* ctx, const u16* wo, const u16* x, const float* mod, float* x1) {
  __shared__ float red[16][2][64];
  int col = blockIdx.x * 64 + (threadIdx.x & 63), g = threadIdx.x >> 6;
  float a0 = 0, a1 = 0;
  for (int k = g; k < 4096; k += 16) { float w = bf2f(wo[(size_t)k * M + col]); a0 += ctx[k] * w; a1 += ctx[4096 + k] * w; }
  red[g][0][threadIdx.x & 63] = a0; red[g][1][threadIdx.x & 63] = a1;
  __syncthreads();
  if (threadIdx.x < 128) { int b = threadIdx.x >> 6, l = threadIdx.x & 63; float v = 0; for (int i = 0; i < 16; ++i) v += red[i][b][l];
    int m = blockIdx.x * 64 + l; x1[b * M + m] = bf2f(x[b * M + m]) + mod[b * 3840 + 1280 + m] * v; }
}

__global__ void k_gu(const float* x1, const float* mod, const u16* wg, const u16* wu, float* hh) {
  __shared__ float xs[2 * M]; __shared__ float h[2][M]; __shared__ float tmp[4]; __shared__ float red[4][4][64];
  for (int i = threadIdx.x; i < 2 * M; i += 256) xs[i] = x1[i];
  __syncthreads();
  ln_mod(xs, mod, 3 * 640, 4 * 640, h, tmp);
  int col = blockIdx.x * 64 + (threadIdx.x & 63), g = threadIdx.x >> 6;
  float a0 = 0, a1 = 0, u0 = 0, u1 = 0;
  for (int k = g; k < M; k += 4) { float w = bf2f(wg[(size_t)k * F + col]); float w2 = bf2f(wu[(size_t)k * F + col]); a0 += h[0][k] * w; a1 += h[1][k] * w; u0 += h[0][k] * w2; u1 += h[1][k] * w2; }
  int l = threadIdx.x & 63;
  red[g][0][l] = a0; red[g][1][l] = a1; red[g][2][l] = u0; red[g][3][l] = u1;
  __syncthreads();
  if (threadIdx.x < 128) { int b = threadIdx.x >> 6; float gg = red[0][b][l] + red[1][b][l] + red[2][b][l] + red[3][b][l]; float uu = red[0][2 + b][l] + red[1][2 + b][l] + red[2][2 + b][l] + red[3][2 + b][l];
    hh[b * F + blockIdx.x * 64 + l] = uu / (1.f + __expf(-gg)); }
}

__global__ void k_down(const float* hh, const u16* wd, const float* x1, const float* mod, u16* out) {
  __shared__ float red[16][2][64];
  int col = blockIdx.x * 64 + (threadIdx.x & 63), g = threadIdx.x >> 6;
  float a0 = 0, a1 = 0;
  for (int k = g; k < F; k += 16) { float w = bf2f(wd[(size_t)k * M + col]); a0 += hh[k] * w; a1 += hh[F + k] * w; }
  red[g][0][threadIdx.x & 63] = a0; red[g][1][threadIdx.x & 63] = a1;
  __syncthreads();
  if (threadIdx.x < 128) { int b = threadIdx.x >> 6, l = threadIdx.x & 63; float v = 0; for (int i = 0; i < 16; ++i) v += red[i][b][l];
    int m = blockIdx.x * 64 + l; out[b * M + m] = f2bf(x1[b * M + m] + mod[b * 3840 + 5 * 640 + m] * v); }
}

extern "C" void kernel_launch(void* const* d_in, const int* in_sizes, int n_in, void* d_out, int out_size, void* d_ws, size_t ws_size, hipStream_t stream) {
  const u16* x = (const u16*)d_in[0]; const u16* ckv = (const u16*)d_in[1]; const u16* wq = (const u16*)d_in[2]; const u16* wuk = (const u16*)d_in[3];
  const u16* wuv = (const u16*)d_in[4]; const u16* wo = (const u16*)d_in[5]; const u16* wg = (const u16*)d_in[7]; const u16* wu = (const u16*)d_in[8];
  const u16* wd = (const u16*)d_in[9]; const u16* c = (const u16*)d_in[10]; const u16* wmod = (const u16*)d_in[11];
  float* ws = (float*)d_ws;
  float* mod = ws; float* q = mod + 8192; float* qa = q + 8192; float* pm = qa + 32768; float* pl = pm + 8192; float* ctx = pl + 8192; float* x1 = ctx + 8192; float* hh = x1 + 2048; float* po = hh + 4096;
  k_mod<<<60, 256, 0, stream>>>(c, wmod, mod);
  k_q<<<64, 256, 0, stream>>>(x, mod, wq, q);
  k_qabs<<<256, 256, 0, stream>>>(q, wuk, qa);
  k_attn<<<256, 256, 0, stream>>>(qa, ckv, po, pm, pl);
  k_ctx<<<64, 256, 0, stream>>>(po, pm, pl, wuv, ctx);
  k_wo<<<10, 1024, 0, stream>>>(ctx, wo, x, mod, x1);
  k_gu<<<27, 256, 0, stream>>>(x1, mod, wg, wu, hh);
  k_down<<<10, 1024, 0, stream>>>(hh, wd, x1, mod, (u16*)d_out);
}
```

```cpp
#include <hip/hip_runtime.h>
#include <stdint.h>

#define LAS __attribute__((address_space(3)))
#define GAS __attribute__((address_space(1)))
typedef unsigned u32;
typedef unsigned short u16;
typedef u32 v4u __attribute__((ext_vector_type(4)));
typedef u32 v2u __attribute__((ext_vector_type(2)));
typedef float f4 __attribute__((ext_vector_type(4)));
typedef float f2 __attribute__((ext_vector_type(2)));
typedef short s4 __attribute__((ext_vector_type(4)));
typedef __bf16 bf8 __attribute__((ext_vector_type(8)));
typedef __bf16 bf2 __attribute__((ext_vector_type(2)));
typedef u16 u16x2 __attribute__((ext_vector_type(2)));
typedef __amdgpu_buffer_rsrc_t rsrc_t;
#define DI __device__ __forceinline__

constexpr int OFF_EPOCH = 0;
constexpr int OFF_S1 = 4096;
constexpr int OFF_CLR = 32768;
constexpr int OFF_S2 = OFF_CLR;
constexpr int OFF_S3 = OFF_S2 + 131072;
constexpr int OFF_S3L = OFF_S3 + 2097152;
constexpr int OFF_S4 = OFF_S3L + 16384;
constexpr int OFF_S5 = OFF_S4 + 655360;
constexpr int OFF_S6 = OFF_S5 + 10240;
constexpr int OFF_MR = OFF_S6 + 552960;
constexpr int CLR_PER_WG = 27;
static_assert(OFF_MR + 20480 <= OFF_CLR + 128 * CLR_PER_WG * 1024, "clear region too small");
constexpr int WS_BYTES = OFF_CLR + 128 * CLR_PER_WG * 1024;
constexpr int OFF_DBG = 8 << 20;
constexpr u32 SENT = 0xFFFFFFFFu;
constexpr u32 SPIN_CAP = 1u << 18;
constexpr int OOB = 0x7ffffff0;

constexpr int L_CK = 0, L_GU = 0, L_WD = 49152, L_QI = 131072, L_PI = 147456, L_EI = 151552, L_LI = 152064, L_XRED = 152576, L_XST = 154624,
              L_H2 = 154880, L_GRED = 157440;
constexpr int L_WQ = 0, L_WK = 81920, L_WO = 0, L_WV = 122880, L_H = 155648, L_QP = 158208, L_CC = 160256, L_CX = 160768, L_YRED = 161280,
              L_YRED2 = 161792;
constexpr int LDS_BYTES = 163840;

DI float bf_lo(u32 v) { return __uint_as_float(v << 16); }
DI float bf_hi(u32 v) { return __uint_as_float(v & 0xffff0000u); }
DI u32 pk(float a, float b) { f2 f = {a, b}; return __builtin_bit_cast(u32, __builtin_convertvector(f, bf2)); }
DI float rbf(float a) { return bf_lo(pk(a, a)); }
template <int CTRL> DI float dpp(float v) { return __int_as_float(__builtin_amdgcn_update_dpp(0, __float_as_int(v), CTRL, 0xf, 0xf, false)); }
template <int N> DI float ror(float v) { return dpp<0x120 + N>(v); }
DI void sw16(float a, float b, float& r0, float& r1) {
  auto r = __builtin_amdgcn_permlane16_swap(__float_as_uint(a), __float_as_uint(b), false, false);
  r0 = __uint_as_float(r[0]); r1 = __uint_as_float(r[1]);
}
DI void sw32(float a, float b, float& r0, float& r1) {
  auto r = __builtin_amdgcn_permlane32_swap(__float_as_uint(a), __float_as_uint(b), false, false);
  r0 = __uint_as_float(r[0]); r1 = __uint_as_float(r[1]);
}
DI float sum16x(float v) { float a, b; sw16(v, v, a, b); return a + b; }
DI float sum32x(float v) { float a, b; sw32(v, v, a, b); return a + b; }
DI float max16x(float v) { float a, b; sw16(v, v, a, b); return fmaxf(a, b); }
DI float max32x(float v) { float a, b; sw32(v, v, a, b); return fmaxf(a, b); }
DI f4 mfma32(v4u a, v4u b, f4 c) { return __builtin_amdgcn_mfma_f32_16x16x32_bf16(__builtin_bit_cast(bf8, a), __builtin_bit_cast(bf8, b), c, 0, 0, 0); }
DI f4 mfma16(v2u a, v2u b, f4 c) { return __builtin_amdgcn_mfma_f32_16x16x16bf16_1k(__builtin_bit_cast(s4, a), __builtin_bit_cast(s4, b), c, 0, 0, 0); }
DI float dot2(u32 a, u32 b, float c) { return __builtin_amdgcn_fdot2_f32_bf16(__builtin_bit_cast(bf2, a), __builtin_bit_cast(bf2, b), c, false); }
DI rsrc_t mkrsrc(const void* p, int bytes) { return __builtin_amdgcn_make_buffer_rsrc((void*)p, 0, bytes, 0x00020000); }
DI v4u ld16(rsrc_t r, int off) { return __builtin_amdgcn_raw_buffer_load_b128(r, off, 0, 16); }
DI v2u ld8(rsrc_t r, int off) { return __builtin_amdgcn_raw_buffer_load_b64(r, off, 0, 16); }
DI u32 ld4(rsrc_t r, int off) { return __builtin_amdgcn_raw_buffer_load_b32(r, off, 0, 16); }
DI void st16(rsrc_t r, int off, v4u v) { __builtin_amdgcn_raw_buffer_store_b128(v, r, off, 0, 16); }
DI void st8(rsrc_t r, int off, v2u v) { __builtin_amdgcn_raw_buffer_store_b64(v, r, off, 0, 16); }
DI void st4(rsrc_t r, int off, u32 v) { __builtin_amdgcn_raw_buffer_store_b32(v, r, off, 0, 16); }
DI v4u ldw16(rsrc_t r, int off) { return __builtin_amdgcn_raw_buffer_load_b128(r, off, 0, 2); }
DI u32 subsat(u32 a, u32 b) { return __builtin_bit_cast(u32, __builtin_elementwise_sub_sat(__builtin_bit_cast(u16x2, a), __builtin_bit_cast(u16x2, b))); }
DI u32 nosent(u32 v) { return v < 0xFFFFFFFEu ? v : 0xFFFFFFFEu; }
DI v4u nosent4(v4u v) { return v4u{nosent(v.x), nosent(v.y), nosent(v.z), nosent(v.w)}; }
DI v4u f4u(f4 v) { return __builtin_bit_cast(v4u, v); }
DI f4 u4f(v4u v) { return __builtin_bit_cast(f4, v); }
DI u32 umax(u32 a, u32 b) { return a > b ? a : b; }
DI u32 umax4(v4u v) { return umax(umax(v.x, v.y), umax(v.z, v.w)); }
DI void dma(rsrc_t r, u32 voff, u32 soff, u32 lds) {
  asm volatile("s_nop 4\n\ts_mov_b32 m0, %0\n\ts_nop 0\n\tbuffer_load_dwordx4 %1, %2, %3 offen nt lds" ::"s"(lds), "v"(voff), "s"(r), "s"(soff) : "memory");
}
#define VMWAIT(n) asm volatile("s_waitcnt vmcnt(" #n ")" ::: "memory")
#define LGKM0() asm volatile("s_waitcnt lgkmcnt(0)" ::: "memory")
#define BAR() do { asm volatile("s_waitcnt lgkmcnt(0)" ::: "memory"); __builtin_amdgcn_s_barrier(); asm volatile("" ::: "memory"); } while (0)
#define UNI(x) __builtin_amdgcn_readfirstlane(x)

#define STAMP(i) do { } while (0)

DI int perm80(int m) {
  const int w = m / 80, r = m - 80 * w;
  return r < 64 ? 80 * w + (r & 32) + 8 * ((r >> 2) & 3) + 4 * ((r >> 4) & 1) + (r & 3) : m;
}
DI int swz16(int row) { return ((row & 7) << 1) | ((row >> 3) & 1); }
DI float silu_r(float v) { return rbf(v / (1.f + __expf(-v))); }

DI void mod_pair(v4u w0, v4u w1, u32 c0, u32 c1, float (&acc)[16]) {
  const u32 s0 = pk(silu_r(bf_lo(c0)), silu_r(bf_hi(c0))), s1 = pk(silu_r(bf_lo(c1)), silu_r(bf_hi(c1)));
#pragma unroll
  for (int k = 0; k < 4; ++k) {
    const u32 lo = __builtin_amdgcn_perm(w1[k], w0[k], 0x05040100u), hi = __builtin_amdgcn_perm(w1[k], w0[k], 0x07060302u);
    acc[2 * k] = dot2(lo, s0, acc[2 * k]); acc[2 * k + 1] = dot2(hi, s0, acc[2 * k + 1]);
    acc[8 + 2 * k] = dot2(lo, s1, acc[8 + 2 * k]); acc[8 + 2 * k + 1] = dot2(hi, s1, acc[8 + 2 * k + 1]);
  }
}
DI void mod_reduce(float (&acc)[16], LAS float* red, int lane) {
  float t[8], u[4];
#pragma unroll
  for (int i = 0; i < 8; ++i) { float a, b; sw32(acc[i], acc[i + 8], a, b); t[i] = a + b; }
#pragma unroll
  for (int i = 0; i < 4; ++i) { float a, b; sw16(t[i], t[i + 4], a, b); u[i] = a + b; }
#pragma unroll
  for (int i = 0; i < 4; ++i) { u[i] += ror<4>(u[i]); u[i] += ror<8>(u[i]); }
  if ((lane & 12) == 0) *(LAS f4*)(red + (lane >> 5) * 32 + (lane & 3) * 8 + ((lane >> 4) & 1) * 4) = f4{u[0], u[1], u[2], u[3]};
}
DI v2u trread(LAS unsigned char* p) { return __builtin_bit_cast(v2u, __builtin_amdgcn_ds_read_tr16_b64_v4i16((LAS s4*)p)); }

DI f4 piece_reduce(f4 v, LAS unsigned char* red, int tid, int lane, int w) {
#pragma unroll
  for (int i = 0; i < 4; ++i) { v[i] += ror<4>(v[i]); v[i] += ror<8>(v[i]); }
  float a, b, c, d, e0, e1, f0;
  sw32(v[0], v[1], a, b); e0 = a + b;
  sw32(v[2], v[3], c, d); e1 = c + d;
  sw16(e0, e1, a, b); f0 = a + b;
  if ((lane & 12) == 0) { const int row = lane >> 4; ((LAS float*)red)[(w * 4 + (lane & 3)) * 4 + ((row & 1) * 2 + (row >> 1))] = f0; }
  BAR();
  f4 r = {0.f, 0.f, 0.f, 0.f};
  if (w == 0) {
    r = *(LAS f4*)(red + (lane & 31) * 16);
#pragma unroll
    for (int i = 0; i < 4; ++i) { r[i] += ror<4>(r[i]); r[i] += ror<8>(r[i]); r[i] = sum16x(r[i]); }
  }
  return r;
}

__global__ void __launch_bounds__(512, 2)
fused_decode(const u16* __restrict__ gx, const u16* __restrict__ gckv, const u16* __restrict__ gwq, const u16* __restrict__ gwuk,
             const u16* __restrict__ gwuv, const u16* __restrict__ gwo, const u16* __restrict__ gwg, const u16* __restrict__ gwu,
             const u16* __restrict__ gwd, const u16* __restrict__ gc, const u16* __restrict__ gwmod, u16* __restrict__ gout,
             unsigned char* __restrict__ ws) {
  __shared__ __attribute__((aligned(16))) unsigned char smem[LDS_BYTES];
  LAS unsigned char* L = (LAS unsigned char*)smem;
  const u32 L0 = (u32)(size_t)L;
  const int tid = threadIdx.x, lane = tid & 63, w = UNI(tid >> 6);
  const int gidx = blockIdx.x, jj = gidx >> 3, idx = (gidx & 7) * 16 + (jj & 15);
  const int li = lane & 15, g = lane >> 4, q4 = (lane >> 2) & 3, p4 = lane & 3;
  const rsrc_t rws = mkrsrc(ws, WS_BYTES);
  STAMP(0);
  const u32 tagv = ld4(rws, OFF_EPOCH) + 1u;

  if (jj >= 16) {
    const int xi = idx;
    const rsrc_t rmod = mkrsrc(gwmod, 640 * 3840 * 2), rc = mkrsrc(gc, 2560), rx = mkrsrc(gx, 2560);
    {
      const int cg = xi / 3, ksp = xi - 3 * cg, rp = tid >> 2, cc = tid & 3;
      const bool act = xi < 120 && rp < (ksp == 2 ? 96 : 112);
      const int row0 = 224 * ksp + 2 * rp, col = (cc >> 1) * 640 + 16 * cg + 8 * (cc & 1);
      const int wo_ = act ? (row0 * 3840 + col) * 2 : OOB;
      const v4u w0 = ldw16(rmod, wo_), w1 = ldw16(rmod, act ? wo_ + 7680 : OOB);
      const u32 c0 = __builtin_amdgcn_raw_buffer_load_b32(rc, act ? row0 * 2 : OOB, 0, 0), c1 = __builtin_amdgcn_raw_buffer_load_b32(rc, act ? 1280 + row0 * 2 : OOB, 0, 0);
      v4u xa = {0, 0, 0, 0}, xb = {0, 0, 0, 0}; u32 xm = 0;
      if (w >= 6) { xa = __builtin_amdgcn_raw_buffer_load_b128(rx, (w - 6) * 1280 + lane * 16, 0, 0); xb = __builtin_amdgcn_raw_buffer_load_b128(rx, lane < 16 ? (w - 6) * 1280 + 1024 + lane * 16 : OOB, 0, 0); }
      if (w == 0) xm = __builtin_amdgcn_raw_buffer_load_b16(rx, xi < 120 ? ((lane >> 5) * 640 + 16 * cg + li) * 2 : OOB, 0, 0);
      for (int i = w; i < CLR_PER_WG; i += 8) st16(rws, OFF_CLR + (xi * CLR_PER_WG + i) * 1024 + lane * 16, v4u{SENT, SENT, SENT, SENT});
      float acc[16];
#pragma unroll
      for (int i = 0; i < 16; ++i) acc[i] = 0.f;
      mod_pair(w0, w1, c0, c1, acc);
      mod_reduce(acc, (LAS float*)(L + L_XRED) + w * 64, lane);
      if (w >= 6) {
        float s = 0.f, ss = 0.f;
#pragma unroll
        for (int k = 0; k < 4; ++k) { float a = bf_lo(xa[k]), b = bf_hi(xa[k]), c = bf_lo(xb[k]), d = bf_hi(xb[k]); s += (a + b) + (c + d); ss += (a * a + b * b) + (c * c + d * d); }
        s += ror<1>(s); s += ror<2>(s); s += ror<4>(s); s += ror<8>(s); s = sum32x(sum16x(s));
        ss += ror<1>(ss); ss += ror<2>(ss); ss += ror<4>(ss); ss += ror<8>(ss); ss = sum32x(sum16x(ss));
        const float mean = s * (1.f / 640.f), var = fmaxf(ss * (1.f / 640.f) - mean * mean, 0.f);
        if (lane == 0) *(LAS f2*)(L + L_XST + (w - 6) * 8) = f2{mean, 1.f / sqrtf(var + 1e-5f)};
      }
      VMWAIT(0);
      BAR();
      if (w == 0) {
        float v = 0.f;
#pragma unroll
        for (int k = 0; k < 8; ++k) v += ((LAS float*)(L + L_XRED))[k * 64 + lane];
        const f2 st = *(LAS f2*)(L + L_XST + (lane >> 5) * 8);
        const float xn = (bf_lo(xm) - st.x) * st.y;
        float sa, ca, h0, h1;
        sw16(v, v, sa, ca);
        const float hp = xn * ca + sa + (ksp == 0 ? xn : 0.f);
        sw32(hp, hp, h0, h1);
        if (lane < 16) st8(rws, OFF_S1 + xi * 128 + lane * 8, v2u{pk(h0, h1), tagv});
      }
    }
    STAMP(1);
    const int xc = xi >> 4, b = xc >> 2, chunk = (xc & 3) * 8 + ((xi & 15) >> 1), hg = xi & 1;
    {
      const rsrc_t rck = mkrsrc(gckv, 2 * 4096 * 512 * 2);
#pragma unroll
      for (int k = 0; k < 16; ++k) {
        const int row = ((k + 8 * hg) & 15) * 8 + w;
        dma(rck, (u32)((lane ^ swz16(row)) * 16), (u32)((b * 4096 + chunk * 128 + row) * 1024), L0 + L_CK + row * 1024);
      }
    }
    if (w < 2) {
      for (u32 sp = 0; sp < SPIN_CAP; ++sp) {
        asm volatile("" ::: "memory");
        const v2u t = ld8(rws, OFF_S1 + tid * 128);
        if (__all(t.y == tagv)) break;
      }
    }
    VMWAIT(0);
    BAR();
    STAMP(2);
    v4u ca_[16], ct_[4][4];
    {
      const int fi = swz16(li);
#pragma unroll
      for (int s = 0; s < 16; ++s) ca_[s] = *(LAS v4u*)(L + L_CK + (16 * w + li) * 1024 + ((s ^ fi) + 16 * g) * 16);
      const int fr = swz16(4 * g + q4);
#pragma unroll
      for (int ct = 0; ct < 4; ++ct) {
        const int ch = 8 * w + 2 * ct + (p4 >> 1);
        LAS unsigned char* base = L + L_CK + (4 * g + q4) * 1024 + ((ch & ~15) | ((ch & 15) ^ fr)) * 16 + 8 * (p4 & 1);
#pragma unroll
        for (int s = 0; s < 4; ++s) {
          const v2u lo = trread(base + (32 * s) * 1024), hi = trread(base + (32 * s + 16) * 1024);
          ct_[ct][s] = v4u{lo.x, lo.y, hi.x, hi.y};
        }
      }
    }
    STAMP(3);
    {
      const int o0 = OFF_S2 + ((b * 2) * 32 + hg * 16) * 1024 + tid * 16;
      v4u a0, a1, b0, b1;
      for (u32 sp = 0; sp < SPIN_CAP; ++sp) {
        asm volatile("" ::: "memory");
        a0 = ld16(rws, o0); a1 = ld16(rws, o0 + 8192); b0 = ld16(rws, o0 + 32768); b1 = ld16(rws, o0 + 32768 + 8192);
        const u32 m = umax(umax(umax4(a0), umax4(a1)), umax(umax4(b0), umax4(b1)));
        if (__all(m != SENT)) break;
      }
      STAMP(4);
      v4u r0, r1;
#pragma unroll
      for (int k = 0; k < 4; ++k) {
        r0[k] = pk(bf_lo(a0[k]) + bf_lo(b0[k]), bf_hi(a0[k]) + bf_hi(b0[k]));
        r1[k] = pk(bf_lo(a1[k]) + bf_lo(b1[k]), bf_hi(a1[k]) + bf_hi(b1[k]));
      }
      *(LAS v4u*)(L + L_QI + w * 1024 + ((lane & 48) | (li ^ swz16(w))) * 16) = r0;
      *(LAS v4u*)(L + L_QI + (w + 8) * 1024 + ((lane & 48) | (li ^ swz16(w + 8))) * 16) = r1;
    }
    BAR();
    {
      const int fi = swz16(li);
      f4 sc = {0.f, 0.f, 0.f, 0.f};
#pragma unroll
      for (int s = 0; s < 16; ++s) {
        const v4u qf = *(LAS v4u*)(L + L_QI + li * 1024 + ((s ^ fi) + 16 * g) * 16);
        sc = mfma32(ca_[s], qf, sc);
      }
      const float mx = max32x(max16x(fmaxf(fmaxf(sc[0], sc[1]), fmaxf(sc[2], sc[3]))));
      const float E = ceilf(mx);
      const float p0 = __builtin_amdgcn_exp2f(sc[0] - E), p1 = __builtin_amdgcn_exp2f(sc[1] - E), p2 = __builtin_amdgcn_exp2f(sc[2] - E), p3 = __builtin_amdgcn_exp2f(sc[3] - E);
      *(LAS v2u*)(L + L_PI + ((w >> 1) * 64 + lane) * 16 + (w & 1) * 8) = v2u{pk(p0, p1), pk(p2, p3)};
      const float ls = sum32x(sum16x((p0 + p1) + (p2 + p3)));
      if (lane < 16) { ((LAS float*)(L + L_EI))[lane * 8 + w] = E; ((LAS float*)(L + L_LI))[lane * 8 + w] = ls; }
      BAR();
      const f4 e0 = *(LAS f4*)(L + L_EI + li * 32), e1 = *(LAS f4*)(L + L_EI + li * 32 + 16);
      v4u pf[4];
#pragma unroll
      for (int s = 0; s < 4; ++s) pf[s] = *(LAS v4u*)(L + L_PI + (s * 64 + lane) * 16);
      const f4 l0 = *(LAS f4*)(L + L_LI + li * 32), l1 = *(LAS f4*)(L + L_LI + li * 32 + 16);
      const float Em = fmaxf(fmaxf(fmaxf(e0[0], e0[1]), fmaxf(e0[2], e0[3])), fmaxf(fmaxf(e1[0], e1[1]), fmaxf(e1[2], e1[3])));
      float dd[8];
#pragma unroll
      for (int t = 0; t < 4; ++t) { dd[t] = Em - e0[t]; dd[4 + t] = Em - e1[t]; }
      f4 o[4];
#pragma unroll
      for (int ct = 0; ct < 4; ++ct) o[ct] = f4{0.f, 0.f, 0.f, 0.f};
#pragma unroll
      for (int s = 0; s < 4; ++s) {
        const u32 ua = (u32)fminf(dd[2 * s], 255.f) * 0x00800080u, ub = (u32)fminf(dd[2 * s + 1], 255.f) * 0x00800080u;
        const v4u pp = {subsat(pf[s].x, ua), subsat(pf[s].y, ua), subsat(pf[s].z, ub), subsat(pf[s].w, ub)};
#pragma unroll
        for (int ct = 0; ct < 4; ++ct) o[ct] = mfma32(ct_[ct][s], pp, o[ct]);
      }
      float lt = 0.f;
#pragma unroll
      for (int t = 0; t < 4; ++t) lt += l0[t] * __builtin_amdgcn_exp2f(-dd[t]) + l1[t] * __builtin_amdgcn_exp2f(-dd[4 + t]);
      const float inv = 1.f / lt;
      const int so = OFF_S3 + xi * 16384 + li * 1024 + w * 128 + g * 16;
#pragma unroll
      for (int u = 0; u < 2; ++u) {
        const f4 x0 = o[2 * u] * inv, x1 = o[2 * u + 1] * inv;
        st16(rws, so + u * 64, nosent4(v4u{pk(x0[0], x0[1]), pk(x0[2], x0[3]), pk(x1[0], x1[1]), pk(x1[2], x1[3])}));
      }
      if (w == 0 && lane < 16) st4(rws, OFF_S3L + xi * 128 + lane * 4, nosent(__float_as_uint(Em + __builtin_amdgcn_logf(lt))));
    }
    STAMP(5);
    const bool gd = xi < 108;
    if (gd) {
      const int f0 = 16 * xi;
      const rsrc_t rg = mkrsrc(gwg, 640 * 1728 * 2), ru = mkrsrc(gwu, 640 * 1728 * 2), rd = mkrsrc(gwd, 1728 * 640 * 2);
      const u32 vo = (u32)((lane >> 1) * 3456 + (lane & 1) * 16);
#pragma unroll
      for (int k = 0; k < 3; ++k) {
        const u32 v = (k == 2 && lane >= 32) ? (u32)OOB : vo;
        dma(rg, v, (u32)(((80 * w + 32 * k) * 1728 + f0) * 2), L0 + L_GU + w * 6144 + k * 1024);
        dma(ru, v, (u32)(((80 * w + 32 * k) * 1728 + f0) * 2), L0 + L_GU + w * 6144 + 3072 + k * 1024);
      }
      for (int k = w; k < 20; k += 8) {
        const int P = 64 * k + lane, row = P / 80, pos = P - 80 * row;
        dma(rd, (u32)(row * 1280 + ((pos & ~15) | ((pos & 15) ^ swz16(row))) * 16), (u32)(f0 * 1280), L0 + L_WD + k * 1024);
      }
    }
    if (xi >= 48) {
      const int mi = xi - 48, blk = mi / 20, cg = mi - 20 * blk, rp = tid >> 2, cc = tid & 3;
      const int col = (2 + blk) * 640 + 32 * cg + 8 * cc;
      v4u w0[3], w1[3]; u32 c0[3], c1[3];
#pragma unroll
      for (int i = 0; i < 3; ++i) {
        const bool act = rp + 128 * i < 320;
        const int row0 = 2 * (rp + 128 * i), wo_ = act ? (row0 * 3840 + col) * 2 : OOB;
        w0[i] = ldw16(rmod, wo_); w1[i] = ldw16(rmod, act ? wo_ + 7680 : OOB);
        c0[i] = __builtin_amdgcn_raw_buffer_load_b32(rc, act ? row0 * 2 : OOB, 0, 0); c1[i] = __builtin_amdgcn_raw_buffer_load_b32(rc, act ? 1280 + row0 * 2 : OOB, 0, 0);
      }
      float acc[16];
#pragma unroll
      for (int i = 0; i < 16; ++i) acc[i] = 0.f;
#pragma unroll
      for (int i = 0; i < 3; ++i) mod_pair(w0[i], w1[i], c0[i], c1[i], acc);
      mod_reduce(acc, (LAS float*)(L + L_XRED) + w * 64, lane);
      BAR();
      if (w == 0) {
        float v = 0.f;
#pragma unroll
        for (int k = 0; k < 8; ++k) v += ((LAS float*)(L + L_XRED))[k * 64 + lane];
        st4(rws, OFF_MR + mi * 256 + lane * 4, nosent(__float_as_uint(rbf(v))));
      }
    }
    STAMP(6);
    if (gd) {
      VMWAIT(0);
      BAR();
      v2u ga_[2][5], da_[5];
      {
        LAS unsigned char* base = L + L_GU + w * 6144 + (4 * g + q4) * 32 + 8 * p4;
#pragma unroll
        for (int m = 0; m < 2; ++m)
#pragma unroll
          for (int rg_ = 0; rg_ < 5; ++rg_) ga_[m][rg_] = trread(base + m * 3072 + rg_ * 512);
        const int row = 4 * g + q4, fr = swz16(row);
#pragma unroll
        for (int t = 0; t < 5; ++t) {
          const int ch = 2 * (5 * w + t) + (p4 >> 1);
          da_[t] = trread(L + L_WD + row * 1280 + ((ch & ~15) | ((ch & 15) ^ fr)) * 16 + 8 * (p4 & 1));
        }
      }
      const int r = tid >> 2, bb = (tid >> 1) & 1, hf = tid & 1, m0 = 8 * r + 4 * hf;
      f4 sb4 = {0.f, 0.f, 0.f, 0.f}, cb4 = sb4, x4 = sb4;
      if (w < 5) {
        const int mo = OFF_MR + (m0 >> 5) * 256 + bb * 128 + (m0 & 31) * 4;
        for (u32 sp = 0; sp < SPIN_CAP; ++sp) {
          asm volatile("" ::: "memory");
          const v4u a = ld16(rws, mo + 20 * 256), c = ld16(rws, mo + 40 * 256);
          sb4 = u4f(a); cb4 = u4f(c);
          if (__all(umax(umax4(a), umax4(c)) != SENT)) break;
        }
        STAMP(7);
        for (u32 sp = 0; sp < SPIN_CAP; ++sp) {
          asm volatile("" ::: "memory");
          const v4u a = ld16(rws, OFF_S5 + r * 128 + (tid & 3) * 16);
          x4 = u4f(a);
          if (__all(umax4(a) != SENT)) break;
        }
        STAMP(8);
        float s = (x4[0] + x4[1]) + (x4[2] + x4[3]), ss = (x4[0] * x4[0] + x4[1] * x4[1]) + (x4[2] * x4[2] + x4[3] * x4[3]);
        s += dpp<0xB1>(s); ss += dpp<0xB1>(ss);
        s += ror<4>(s); ss += ror<4>(ss); s += ror<8>(s); ss += ror<8>(ss);
        float a0, a1; sw32(s, ss, a0, a1); a0 += a1;
        a0 = sum16x(a0);
        if ((lane & 29) == 0) ((LAS float*)(L + L_XST))[((lane >> 1) & 1) * 16 + (lane >> 5) * 8 + w] = a0;
      }
      BAR();
      if (w < 5) {
        const f4 s0 = *(LAS f4*)(L + L_XST + bb * 64), q0 = *(LAS f4*)(L + L_XST + bb * 64 + 32);
        const float s1 = *(LAS float*)(L + L_XST + bb * 64 + 16), q1 = *(LAS float*)(L + L_XST + bb * 64 + 48);
        const float mean = ((s0[0] + s0[1]) + (s0[2] + s0[3]) + s1) * (1.f / 640.f);
        const float var = fmaxf(((q0[0] + q0[1]) + (q0[2] + q0[3]) + q1) * (1.f / 640.f) - mean * mean, 0.f);
        const float rs = 1.f / sqrtf(var + 1e-5f);
        f4 h;
#pragma unroll
        for (int i = 0; i < 4; ++i) h[i] = (x4[i] - mean) * rs * (1.f + cb4[i]) + sb4[i];
        *(LAS v2u*)(L + L_H2 + bb * 1280 + perm80(m0) * 2) = v2u{pk(h[0], h[1]), pk(h[2], h[3])};
      }
      BAR();
      {
        LAS unsigned char* hb = L + L_H2 + (li & 1) * 1280 + 160 * w;
        const v4u h0 = *(LAS v4u*)(hb + 16 * g), h1 = *(LAS v4u*)(hb + 64 + 16 * g);
        const v2u h2 = *(LAS v2u*)(hb + 128 + 8 * g);
#pragma unroll
        for (int m = 0; m < 2; ++m) {
          f4 a = {0.f, 0.f, 0.f, 0.f};
          a = mfma32(v4u{ga_[m][0].x, ga_[m][0].y, ga_[m][1].x, ga_[m][1].y}, h0, a);
          a = mfma32(v4u{ga_[m][2].x, ga_[m][2].y, ga_[m][3].x, ga_[m][3].y}, h1, a);
          a = mfma32(v4u{ga_[m][4].x, ga_[m][4].y, 0u, 0u}, v4u{h2.x, h2.y, 0u, 0u}, a);
          if (li < 2) *(LAS f4*)(L + L_GRED + (m * 64 + g * 16 + 2 * w + li) * 16) = a;
        }
      }
      BAR();
      {
        f4 gg = *(LAS f4*)(L + L_GRED + lane * 16), uu = *(LAS f4*)(L + L_GRED + (64 + lane) * 16);
        float hh[4];
#pragma unroll
        for (int i = 0; i < 4; ++i) {
          gg[i] += ror<2>(gg[i]); gg[i] += ror<4>(gg[i]); gg[i] += ror<8>(gg[i]);
          uu[i] += ror<2>(uu[i]); uu[i] += ror<4>(uu[i]); uu[i] += ror<8>(uu[i]);
          hh[i] = uu[i] / (1.f + __expf(-gg[i]));
        }
        const v2u hb = {pk(hh[0], hh[1]), pk(hh[2], hh[3])};
        const int so = OFF_S6 + xi * 5120 + 5 * w * 128 + (g >> 1) * 64 + li * 32 + (g & 1) * 16;
#pragma unroll
        for (int t = 0; t < 5; ++t) {
          const f4 o = mfma16(da_[t], hb, f4{0.f, 0.f, 0.f, 0.f});
          if (li < 2) st16(rws, so + t * 128, nosent4(f4u(o)));
        }
      }
      STAMP(9);
    }
  } else {
    const int yi = idx, h = yi >> 2, sub = yi & 3, dh = sub & 1, ch = sub >> 1;
    {
      const rsrc_t rq = mkrsrc(gwq, 640 * 4096 * 2), rk = mkrsrc(gwuk, 512 * 4096 * 2);
      const u32 vq = (u32)((lane >> 3) * 8192 + ((lane & 7) ^ (g << 1)) * 16);
      const int k0 = 5 * ch, k1 = 5 - k0;
#pragma unroll
      for (int k = 0; k < 5; ++k) dma(rq, vq, (u32)(((80 * w + 8 * (k0 + k)) * 4096 + h * 128 + dh * 64) * 2), L0 + L_WQ + w * 10240 + (k0 + k) * 1024);
#pragma unroll
      for (int k = 0; k < 4; ++k) {
        const int ii = 8 * (k & 1) + (lane >> 3), cl = 8 * (ii >> 2) + 4 * (k >> 1) + (ii & 3);
        dma(rk, (u32)(cl * 8192 + ((lane & 7) ^ ((ii >> 1) & 7)) * 16), (u32)(((ch * 256 + 32 * w) * 4096 + h * 128 + dh * 64) * 2), L0 + L_WK + w * 4096 + k * 1024);
      }
#pragma unroll
      for (int k = 0; k < 5; ++k) dma(rq, vq, (u32)(((80 * w + 8 * (k1 + k)) * 4096 + h * 128 + dh * 64) * 2), L0 + L_WQ + w * 10240 + (k1 + k) * 1024);
    }
    if (w < 6) {
      const int cg = tid >> 3, c8 = tid & 7;
      const int o0 = OFF_S1 + (w < 5 ? 3 * cg : 80 + cg) * 128 + c8 * 16, st = w < 5 ? 128 : 0;
      v4u a0, a1, a2;
      for (u32 sp = 0; sp < SPIN_CAP; ++sp) {
        asm volatile("" ::: "memory");
        a0 = ld16(rws, o0); a1 = ld16(rws, o0 + st); a2 = ld16(rws, o0 + 2 * st);
        const bool ok = (a0.y == tagv) & (a0.w == tagv) & (a1.y == tagv) & (a1.w == tagv) & (a2.y == tagv) & (a2.w == tagv);
        if (__all(ok)) break;
      }
      if (w < 5) {
        const int pos = perm80(16 * cg + 2 * c8);
        *(LAS u32*)(L + L_H + pos * 2) = pk(bf_lo(a0.x) + bf_lo(a1.x) + bf_lo(a2.x), bf_lo(a0.z) + bf_lo(a1.z) + bf_lo(a2.z));
        *(LAS u32*)(L + L_H + 1280 + pos * 2) = pk(bf_hi(a0.x) + bf_hi(a1.x) + bf_hi(a2.x), bf_hi(a0.z) + bf_hi(a1.z) + bf_hi(a2.z));
      }
    }
    STAMP(1);
    VMWAIT(0);
    {
      v2u qa[4][5]; v4u ka[2][2];
      const int fq = (2 * g + (q4 >> 1)) & 3;
#pragma unroll
      for (int nt = 0; nt < 4; ++nt) {
        LAS unsigned char* base = L + L_WQ + w * 10240 + (4 * g + q4) * 128 + (((2 * nt) ^ (fq << 1)) + (p4 >> 1)) * 16 + 8 * (p4 & 1);
#pragma unroll
        for (int rg_ = 0; rg_ < 5; ++rg_) qa[nt][rg_] = trread(base + rg_ * 2048);
      }
      const int fk = (li >> 1) & 7;
#pragma unroll
      for (int s = 0; s < 2; ++s) {
        LAS unsigned char* b0 = L + L_WK + w * 4096 + li * 128 + ((4 * s + (g >> 1)) ^ fk) * 16 + 8 * (g & 1);
        LAS unsigned char* b1 = L + L_WK + w * 4096 + li * 128 + ((4 * s + 2 + (g >> 1)) ^ fk) * 16 + 8 * (g & 1);
#pragma unroll
        for (int t = 0; t < 2; ++t) { const v2u lo = *(LAS v2u*)(b0 + t * 2048), hi = *(LAS v2u*)(b1 + t * 2048); ka[t][s] = v4u{lo.x, lo.y, hi.x, hi.y}; }
      }
      BAR();
      STAMP(2);
      LAS unsigned char* hb = L + L_H + (li & 1) * 1280 + 160 * w;
      const v4u h0 = *(LAS v4u*)(hb + 16 * g), h1 = *(LAS v4u*)(hb + 64 + 16 * g);
      const v2u h2 = *(LAS v2u*)(hb + 128 + 8 * g);
      f4 a[4];
#pragma unroll
      for (int nt = 0; nt < 4; ++nt) {
        a[nt] = mfma32(v4u{qa[nt][0].x, qa[nt][0].y, qa[nt][1].x, qa[nt][1].y}, h0, f4{0.f, 0.f, 0.f, 0.f});
        a[nt] = mfma32(v4u{qa[nt][2].x, qa[nt][2].y, qa[nt][3].x, qa[nt][3].y}, h1, a[nt]);
        a[nt] = mfma32(v4u{qa[nt][4].x, qa[nt][4].y, 0u, 0u}, v4u{h2.x, h2.y, 0u, 0u}, a[nt]);
      }
      if (li < 2) {
#pragma unroll
        for (int s = 0; s < 2; ++s)
          *(LAS v4u*)(L + L_QP + (s * 64 + g * 16 + 2 * w + li) * 16) =
              v4u{pk(a[2 * s][0], a[2 * s][1]), pk(a[2 * s][2], a[2 * s][3]), pk(a[2 * s + 1][0], a[2 * s + 1][1]), pk(a[2 * s + 1][2], a[2 * s + 1][3])};
      }
      BAR();
      const v4u q0 = *(LAS v4u*)(L + L_QP + lane * 16), q1 = *(LAS v4u*)(L + L_QP + (64 + lane) * 16);
      f4 d[2];
#pragma unroll
      for (int t = 0; t < 2; ++t) {
        d[t] = mfma32(ka[t][0], q0, f4{0.f, 0.f, 0.f, 0.f});
        d[t] = mfma32(ka[t][1], q1, d[t]);
      }
      constexpr float SC = 0.08838834764831845f * 1.4426950408889634f;
#pragma unroll
      for (int t = 0; t < 2; ++t)
#pragma unroll
        for (int i = 0; i < 4; ++i) { float v = d[t][i]; v += ror<2>(v); v += ror<4>(v); v += ror<8>(v); d[t][i] = v * SC; }
      if (li < 2)
        st16(rws, OFF_S2 + (((li * 2 + dh) * 32 + h) * 512 + ch * 256 + 32 * w + 8 * g) * 2,
             nosent4(v4u{pk(d[0][0], d[0][1]), pk(d[0][2], d[0][3]), pk(d[1][0], d[1][1]), pk(d[1][2], d[1][3])}));
    }
    STAMP(3);
    const int cq = sub;
    v4u va[4], oa[4][5];
    {
      const rsrc_t rv = mkrsrc(gwuv, 512 * 4096 * 2), ro = mkrsrc(gwo, 4096 * 640 * 2);
      u32 vo[5];
#pragma unroll
      for (int k = 0; k < 5; ++k) {
        const int P = 64 * (5 * w + k) + lane, row = P / 80, pos = P - 80 * row;
        vo[k] = (u32)(row * 1280 + ((pos & ~15) | ((pos & 15) ^ swz16(row & 15))) * 16);
      }
#pragma unroll
      for (int k = 0; k < 4; ++k) {
        const int row = 4 * (4 * w + k) + g;
        dma(rv, (u32)(g * 8192 + (li ^ swz16(row & 15)) * 16), (u32)(((cq * 128 + 4 * (4 * w + k)) * 4096 + h * 128) * 2), L0 + L_WV + (4 * w + k) * 1024);
      }
#pragma unroll
      for (int i = 0; i < 3; ++i)
#pragma unroll
        for (int k = 0; k < 5; ++k) dma(ro, vo[k], (u32)((h * 128 + 32 * ((cq + i) & 3)) * 1280), L0 + L_WO + i * 40960 + (5 * w + k) * 1024);
      const int rr = 4 * g + q4, fr = swz16(rr);
      LAS unsigned char* ob[5];
#pragma unroll
      for (int t = 0; t < 5; ++t) {
        const int c_ = 2 * (5 * w + t) + (p4 >> 1);
        ob[t] = L + L_WO + rr * 1280 + ((c_ & ~15) | ((c_ & 15) ^ fr)) * 16 + 8 * (p4 & 1);
      }
      VMWAIT(10);
      BAR();
      {
        LAS unsigned char* vb = L + L_WV + rr * 256 + ((2 * w + (p4 >> 1)) ^ fr) * 16 + 8 * (p4 & 1);
#pragma unroll
        for (int k = 0; k < 4; ++k) { const v2u lo = trread(vb + (32 * k) * 256), hi = trread(vb + (32 * k + 16) * 256); va[k] = v4u{lo.x, lo.y, hi.x, hi.y}; }
      }
#pragma unroll
      for (int t = 0; t < 5; ++t) { const v2u lo = trread(ob[t]), hi = trread(ob[t] + 16 * 1280); oa[0][t] = v4u{lo.x, lo.y, hi.x, hi.y}; }
      BAR();
#pragma unroll
      for (int k = 0; k < 5; ++k) dma(ro, vo[k], (u32)((h * 128 + 32 * ((cq + 3) & 3)) * 1280), L0 + L_WO + (5 * w + k) * 1024);
      VMWAIT(10);
      BAR();
#pragma unroll
      for (int t = 0; t < 5; ++t) { const v2u lo = trread(ob[t] + 40960), hi = trread(ob[t] + 40960 + 16 * 1280); oa[1][t] = v4u{lo.x, lo.y, hi.x, hi.y}; }
      VMWAIT(5);
      BAR();
#pragma unroll
      for (int t = 0; t < 5; ++t) { const v2u lo = trread(ob[t] + 81920), hi = trread(ob[t] + 81920 + 16 * 1280); oa[2][t] = v4u{lo.x, lo.y, hi.x, hi.y}; }
      VMWAIT(0);
      BAR();
#pragma unroll
      for (int t = 0; t < 5; ++t) { const v2u lo = trread(ob[t]), hi = trread(ob[t] + 16 * 1280); oa[3][t] = v4u{lo.x, lo.y, hi.x, hi.y}; }
    }
    STAMP(4);
    {
      const int b = w >> 2, og = w & 3;
      const int xa = (b * 4 + (li >> 3)) * 16 + (li & 7) * 2 + (h >> 4);
      const int oa_ = OFF_S3 + xa * 16384 + (h & 15) * 1024 + cq * 256 + (4 * og + g) * 16, ol = OFF_S3L + xa * 128 + (h & 15) * 4;
      v4u pa, pb; u32 la, lb;
      for (u32 sp = 0; sp < SPIN_CAP; ++sp) {
        asm volatile("" ::: "memory");
        pa = ld16(rws, oa_); pb = ld16(rws, oa_ + 32 * 16384); la = ld4(rws, ol); lb = ld4(rws, ol + 32 * 128);
        if (__all(umax(umax(umax4(pa), umax4(pb)), umax(la, lb)) != SENT)) break;
      }
      STAMP(5);
      const float fa = __uint_as_float(la), fb = __uint_as_float(lb);
      float m = fmaxf(fa, fb);
      m = fmaxf(m, ror<1>(m)); m = fmaxf(m, ror<2>(m)); m = fmaxf(m, ror<4>(m)); m = fmaxf(m, ror<8>(m));
      const float wa = __builtin_amdgcn_exp2f(fa - m), wb = __builtin_amdgcn_exp2f(fb - m);
      float S = wa + wb;
      S += ror<1>(S); S += ror<2>(S); S += ror<4>(S); S += ror<8>(S);
      float c[8];
#pragma unroll
      for (int k = 0; k < 4; ++k) { c[2 * k] = wa * bf_lo(pa[k]) + wb * bf_lo(pb[k]); c[2 * k + 1] = wa * bf_hi(pa[k]) + wb * bf_hi(pb[k]); }
      const float inv = 1.f / S;
#pragma unroll
      for (int k = 0; k < 8; ++k) { c[k] += ror<1>(c[k]); c[k] += ror<2>(c[k]); c[k] += ror<4>(c[k]); c[k] += ror<8>(c[k]); c[k] *= inv; }
      if (li == 0) *(LAS v4u*)(L + L_CC + (b * 16 + 4 * og + g) * 16) = v4u{pk(c[0], c[1]), pk(c[2], c[3]), pk(c[4], c[5]), pk(c[6], c[7])};
    }
    BAR();
    {
      f4 a = {0.f, 0.f, 0.f, 0.f};
#pragma unroll
      for (int k = 0; k < 4; ++k) a = mfma32(va[k], *(LAS v4u*)(L + L_CC + ((li & 1) * 16 + 4 * k + g) * 16), a);
      if (li < 2) *(LAS v2u*)(L + L_CX + (li * 16 + (w >> 1) * 4 + g) * 16 + (w & 1) * 8) = v2u{pk(a[0], a[1]), pk(a[2], a[3])};
    }
    BAR();
    {
      f4 o[5];
#pragma unroll
      for (int t = 0; t < 5; ++t) o[t] = f4{0.f, 0.f, 0.f, 0.f};
#pragma unroll
      for (int i = 0; i < 4; ++i) {
        const v4u cx = *(LAS v4u*)(L + L_CX + ((li & 1) * 16 + ((cq + i) & 3) * 4 + g) * 16);
#pragma unroll
        for (int t = 0; t < 5; ++t) o[t] = mfma32(oa[i][t], cx, o[t]);
      }
      const int so = OFF_S4 + yi * 5120 + 5 * w * 128 + (g >> 1) * 64 + li * 32 + (g & 1) * 16;
#pragma unroll
      for (int t = 0; t < 5; ++t)
        if (li < 2) st16(rws, so + t * 128, nosent4(f4u(o[t])));
    }
    STAMP(6);
    if (yi < 80) {
      const int r = yi;
      const int bb = (lane >> 1) & 1, m0 = 8 * r + 4 * (lane & 1);
      f4 ga4 = {0.f, 0.f, 0.f, 0.f}, gb4 = ga4, x4 = ga4;
      if (w == 0) {
        const rsrc_t rx = mkrsrc(gx, 2560);
        const v2u xv = __builtin_amdgcn_raw_buffer_load_b64(rx, (bb * 640 + m0) * 2, 0, 0);
        x4 = f4{bf_lo(xv.x), bf_hi(xv.x), bf_lo(xv.y), bf_hi(xv.y)};
        const int mo = OFF_MR + (m0 >> 5) * 256 + bb * 128 + (m0 & 31) * 4;
        for (u32 sp = 0; sp < SPIN_CAP; ++sp) {
          asm volatile("" ::: "memory");
          const v4u a = ld16(rws, mo), c = ld16(rws, mo + 60 * 256);
          ga4 = u4f(a); gb4 = u4f(c);
          if (__all(umax(umax4(a), umax4(c)) != SENT)) break;
        }
      }
      v4u a;
      for (u32 sp = 0; sp < SPIN_CAP; ++sp) {
        asm volatile("" ::: "memory");
        a = ld16(rws, OFF_S4 + (tid >> 2) * 5120 + r * 64 + (tid & 3) * 16);
        if (__all(umax4(a) != SENT)) break;
      }
      STAMP(7);
      f4 x1 = piece_reduce(u4f(a), L + L_YRED, tid, lane, w);
      if (w == 0) {
        x1 = x4 + ga4 * x1;
        if (lane < 4) st16(rws, OFF_S5 + r * 128 + lane * 16, nosent4(f4u(x1)));
      }
      STAMP(8);
      const bool pact = (tid >> 2) < 108;
      for (u32 sp = 0; sp < SPIN_CAP; ++sp) {
        asm volatile("" ::: "memory");
        a = ld16(rws, pact ? OFF_S6 + (tid >> 2) * 5120 + r * 64 + (tid & 3) * 16 : OOB);
        if (__all(umax4(a) != SENT)) break;
      }
      STAMP(9);
      f4 y = piece_reduce(u4f(a), L + L_YRED2, tid, lane, w);
      if (w == 0) {
        y = x1 + gb4 * y;
        if (lane < 4) *(GAS v2u*)((GAS u16*)gout + bb * 640 + m0) = v2u{pk(y[0], y[1]), pk(y[2], y[3])};
        if (r == 0 && lane == 0) st4(rws, OFF_EPOCH, tagv);
      }
      STAMP(10);
    }
  }
}

extern "C" void kernel_launch(void* const* d_in, const int* in_sizes, int n_in, void* d_out, int out_size, void* d_ws, size_t ws_size,
                              hipStream_t stream) {
  fused_decode<<<256, 512, 0, stream>>>((const u16*)d_in[0], (const u16*)d_in[1], (const u16*)d_in[2], (const u16*)d_in[3], (const u16*)d_in[4],
                                        (const u16*)d_in[5], (const u16*)d_in[7], (const u16*)d_in[8], (const u16*)d_in[9], (const u16*)d_in[10],
                                        (const u16*)d_in[11], (u16*)d_out, (unsigned char*)d_ws);
}
```

```cpp
#include <hip/hip_runtime.h>
#include <stdint.h>

#define LAS __attribute__((address_space(3)))
#define GAS __attribute__((address_space(1)))
typedef unsigned u32;
typedef unsigned short u16;
typedef u32 v4u __attribute__((ext_vector_type(4)));
typedef u32 v2u __attribute__((ext_vector_type(2)));
typedef float f4 __attribute__((ext_vector_type(4)));
typedef float f2 __attribute__((ext_vector_type(2)));
typedef short s4 __attribute__((ext_vector_type(4)));
typedef __bf16 bf8 __attribute__((ext_vector_type(8)));
typedef __bf16 bf2 __attribute__((ext_vector_type(2)));
typedef __amdgpu_buffer_rsrc_t rsrc_t;
#define DI __device__ __forceinline__

constexpr int OFF_S1 = 0;
constexpr int OFF_S2 = 262144;
constexpr int OFF_S3F = 393216;
constexpr int OFF_S5 = 917504;
constexpr int OFF_S3 = 1 << 20;
constexpr int OFF_S4 = 3 << 20;
constexpr int OFF_S6 = 4 << 20;
constexpr int OFF_EPOCH = 10 << 20;
constexpr int OFF_TAB = OFF_EPOCH + 4096;
constexpr int OFF_MR = OFF_EPOCH + 16384;
constexpr int WS_BYTES = OFF_MR + 65536;
constexpr u32 SENT = 0xFFFFFFFFu;
constexpr u32 SPIN_CAP = 1u << 18;
constexpr int OOB = 0x7ff00000;
#define GSLEEP 2
#define YDELAY 6
#define XDELAY 6
#define D4 5
#define D6 10
#define S1SLEEP 3
#define GD_EARLY 0
#define TLBW 1

constexpr int L_CK = 0, L_GU = 0, L_WD = 49152, L_QI = 131072, L_PI = 147456, L_EI = 151552, L_MW = 0, L_MS = 28672, L_XST = 156672,
              L_H2 = 81920, L_GRED = 90112;
constexpr int L_WQ = 0, L_WK = 81920, L_WO = 0, L_WV = 81920, L_YMW = 114688, L_H = 143360, L_QP = 145920, L_CC = 147968, L_CX = 148992,
              L_YRED = 149504, L_YRED2 = 150016, L_YMS = 150528;
constexpr int L_SCR = 163328;
constexpr int LDS_BYTES = 163840;

DI float bf_lo(u32 v) { return __uint_as_float(v << 16); }
DI float bf_hi(u32 v) { return __uint_as_float(v & 0xffff0000u); }
DI u32 pk(float a, float b) { f2 f = {a, b}; return __builtin_bit_cast(u32, __builtin_convertvector(f, bf2)); }
DI float rbf(float a) { return bf_lo(pk(a, a)); }
template <int CTRL> DI float dpp(float v) { return __int_as_float(__builtin_amdgcn_update_dpp(0, __float_as_int(v), CTRL, 0xf, 0xf, false)); }
template <int N> DI float ror(float v) { return dpp<0x120 + N>(v); }
DI void sw16(float a, float b, float& r0, float& r1) {
  auto r = __builtin_amdgcn_permlane16_swap(__float_as_uint(a), __float_as_uint(b), false, false);
  r0 = __uint_as_float(r[0]); r1 = __uint_as_float(r[1]);
}
DI void sw32(float a, float b, float& r0, float& r1) {
  auto r = __builtin_amdgcn_permlane32_swap(__float_as_uint(a), __float_as_uint(b), false, false);
  r0 = __uint_as_float(r[0]); r1 = __uint_as_float(r[1]);
}
DI float sum16x(float v) { float a, b; sw16(v, v, a, b); return a + b; }
DI float sum32x(float v) { float a, b; sw32(v, v, a, b); return a + b; }
DI float max16x(float v) { float a, b; sw16(v, v, a, b); return fmaxf(a, b); }
DI float max32x(float v) { float a, b; sw32(v, v, a, b); return fmaxf(a, b); }
DI float vmax(float a, float b) { float r; asm("v_max_f32 %0, %1, %2" : "=v"(r) : "v"(a), "v"(b)); return r; }
DI float vmax3(float a, float b, float c) { float r; asm("v_max3_f32 %0, %1, %2, %3" : "=v"(r) : "v"(a), "v"(b), "v"(c)); return r; }
DI float max16v(float v) { float a, b; sw16(v, v, a, b); return vmax(a, b); }
DI float max32v(float v) { float a, b; sw32(v, v, a, b); return vmax(a, b); }
#define DPA(n, r) "v_add_f32_dpp %" #n ", %" #n ", %" #n " row_ror:" #r " row_mask:0xf bank_mask:0xf\n\t"
#define DPA4(r) DPA(0, r) DPA(1, r) DPA(2, r) DPA(3, r)
#define DPA8(r) DPA4(r) DPA(4, r) DPA(5, r) DPA(6, r) DPA(7, r)
#define DPA10(r) DPA8(r) DPA(8, r) DPA(9, r)
DI void red248_8(f4& a, f4& b) {
  float a0 = a[0], a1 = a[1], a2 = a[2], a3 = a[3], b0 = b[0], b1 = b[1], b2 = b[2], b3 = b[3];
  asm("s_nop 1\n\t" DPA8(2) DPA8(4) DPA8(8) : "+v"(a0), "+v"(a1), "+v"(a2), "+v"(a3), "+v"(b0), "+v"(b1), "+v"(b2), "+v"(b3));
  a = f4{a0, a1, a2, a3}; b = f4{b0, b1, b2, b3};
}
DI void red248_10(f4& a, f4& b, f2& c) {
  float a0 = a[0], a1 = a[1], a2 = a[2], a3 = a[3], b0 = b[0], b1 = b[1], b2 = b[2], b3 = b[3], c0 = c[0], c1 = c[1];
  asm("s_nop 1\n\t" DPA10(2) DPA10(4) DPA10(8) : "+v"(c0), "+v"(c1), "+v"(a0), "+v"(a1), "+v"(a2), "+v"(a3), "+v"(b0), "+v"(b1), "+v"(b2), "+v"(b3));
  a = f4{a0, a1, a2, a3}; b = f4{b0, b1, b2, b3}; c = f2{c0, c1};
}
DI void red48_4(f4& a) {
  float a0 = a[0], a1 = a[1], a2 = a[2], a3 = a[3];
  asm("s_nop 1\n\t" DPA4(4) DPA4(8) : "+v"(a0), "+v"(a1), "+v"(a2), "+v"(a3));
  a = f4{a0, a1, a2, a3};
}
DI v4u subsat4(v4u p, u32 ua, u32 ub) {
  u32 x = p.x, y = p.y, z = p.z, w = p.w;
  asm("v_pk_sub_u16 %0, %0, %4 op_sel_hi:[1,0] clamp\n\tv_pk_sub_u16 %1, %1, %4 op_sel_hi:[1,0] clamp\n\tv_pk_sub_u16 %2, %2, %5 op_sel_hi:[1,0] clamp\n\t"
      "v_pk_sub_u16 %3, %3, %5 op_sel_hi:[1,0] clamp\n\ts_nop 1" : "+v"(x), "+v"(y), "+v"(z), "+v"(w) : "v"(ua), "v"(ub));
  return v4u{x, y, z, w};
}
DI f4 mfma32(v4u a, v4u b, f4 c) { return __builtin_amdgcn_mfma_f32_16x16x32_bf16(__builtin_bit_cast(bf8, a), __builtin_bit_cast(bf8, b), c, 0, 0, 0); }
DI f4 mfma16(v2u a, v2u b, f4 c) { return __builtin_amdgcn_mfma_f32_16x16x16bf16_1k(__builtin_bit_cast(s4, a), __builtin_bit_cast(s4, b), c, 0, 0, 0); }
DI float dot2(u32 a, u32 b, float c) { return __builtin_amdgcn_fdot2_f32_bf16(__builtin_bit_cast(bf2, a), __builtin_bit_cast(bf2, b), c, false); }
DI rsrc_t mkrsrc(const void* p, int bytes) { return __builtin_amdgcn_make_buffer_rsrc((void*)p, 0, bytes, 0x00020000); }
DI v4u ld16(rsrc_t r, int off) { return __builtin_amdgcn_raw_buffer_load_b128(r, off, 0, 16); }
DI v2u ld8(rsrc_t r, int off) { return __builtin_amdgcn_raw_buffer_load_b64(r, off, 0, 16); }
DI u32 ld4(rsrc_t r, int off) { return __builtin_amdgcn_raw_buffer_load_b32(r, off, 0, 16); }
DI void st16(rsrc_t r, int off, v4u v) { __builtin_amdgcn_raw_buffer_store_b128(v, r, off, 0, 16); }
DI void st4(rsrc_t r, int off, u32 v) { __builtin_amdgcn_raw_buffer_store_b32(v, r, off, 0, 16); }
DI u32 ne2(u32 a, u32 b, u32 t) { return __builtin_amdgcn_bitop3_b32(a, b, t, 0x7E); }
DI u32 ne4(v4u a, u32 t) { return ne2(a.y, a.w, t); }
#define CWAIT() __builtin_amdgcn_s_waitcnt(0x0F70)
DI u32 nosent(u32 v) { return v < 0xFFFFFFFEu ? v : 0xFFFFFFFEu; }
DI v4u nosent4(v4u v) { return v4u{nosent(v.x), nosent(v.y), nosent(v.z), nosent(v.w)}; }
DI u32 umax(u32 a, u32 b) { return a > b ? a : b; }
DI u32 umax4(v4u v) { return umax(umax(v.x, v.y), umax(v.z, v.w)); }
DI void dma(rsrc_t r, u32 voff, u32 soff, u32 lds) {
  asm volatile("s_nop 4\n\ts_mov_b32 m0, %0\n\ts_nop 0\n\tbuffer_load_dwordx4 %1, %2, %3 offen nt lds" ::"s"(lds), "v"(voff), "s"(r), "s"(soff) : "memory");
}
DI void dmas(rsrc_t r, u32 voff, u32 soff, u32 lds) {
  asm volatile("s_nop 4\n\ts_mov_b32 m0, %0\n\ts_nop 0\n\tbuffer_load_dwordx4 %1, %2, %3 offen lds" ::"s"(lds), "v"(voff), "s"(r), "s"(soff) : "memory");
}
DI void dmah(rsrc_t r, u32 voff, u32 soff, u32 lds) {
  asm volatile("s_nop 4\n\ts_mov_b32 m0, %0\n\ts_nop 0\n\tbuffer_load_dwordx4 %1, %2, %3 offen lds" ::"s"(lds), "v"(voff), "s"(r), "s"(soff) : "memory");
}
#define STGC(x) 0
#define EARLYC 8
#define STG(x) (x)
DI void ld4sh(u32& d, rsrc_t r, int off) { asm volatile("s_nop 4\n\tbuffer_load_dword %0, %1, %2, 0 offen sc1" : "=v"(d) : "v"(off), "s"(r) : "memory"); }
#define VMWAIT(n) asm volatile("s_waitcnt vmcnt(" #n ")" ::: "memory")
#define LGKM0() asm volatile("s_waitcnt lgkmcnt(0)" ::: "memory")
#define BAR() do { asm volatile("s_waitcnt lgkmcnt(0)" ::: "memory"); __builtin_amdgcn_s_barrier(); asm volatile("" ::: "memory"); } while (0)
#define UNI(x) __builtin_amdgcn_readfirstlane(x)
#define PIN(x) asm volatile("" : "+v"(x))
#define POLL_BEGIN _Pragma("nounroll") for (u32 sp_ = SPIN_CAP; sp_; --sp_) { asm volatile("" ::: "memory");
#define POLL_END(bad) if (__builtin_amdgcn_ballot_w64(bad) == 0) break; }
template <bool TAGGED> DI void gate(rsrc_t r, int off, u32 tag, int lane) {
  const int o = (lane & 15) == 0 ? off : OOB;
#pragma nounroll
  for (u32 sp_ = SPIN_CAP; sp_; --sp_) {
    asm volatile("" ::: "memory");
    const u32 v = ld4(r, o);
    if (__builtin_amdgcn_ballot_w64((lane & 15) == 0 && (TAGGED ? v == tag : v != SENT)) != 0) break;
    __builtin_amdgcn_s_sleep(GSLEEP);
  }
}
DI int PH0(int o) { return ((o >> 13) << 14) | (o & 8191); }
DI int PHS(int o, u32 sbit, u32 smask) { const int ph = PH0(o); return ph | (((sbit ^ (smask >> (8 + (ph >> 21)))) & 1u) << 13); }
DI int PH(int o, int v, u32 smask) { return PHS(o, smask >> v, smask); }
DI u32 tab_decode(u32 pay, int lane) {
  const unsigned long long sb = __builtin_amdgcn_ballot_w64(pay & 1u), vb = __builtin_amdgcn_ballot_w64(pay & 2u);
  u32 m = 0;
#pragma unroll
  for (int v = 0; v < 8; ++v) m |= ((u32)(sb >> (8 * v)) & 1u) << v;
  const int pg = ((lane >> 3) + (lane & 7)) % 5;
  const u32 glob = __builtin_popcountll(vb ^ __builtin_amdgcn_ballot_w64(pg & 1)) > 32 ? 1u : 0u;
#pragma unroll
  for (int p = 0; p < 5; ++p) {
    const unsigned long long in = __builtin_amdgcn_ballot_w64(pg == p);
    const int n = __builtin_popcountll(in), c = __builtin_popcountll(vb & in);
    m |= (4 * c >= 3 * n ? 1u : 4 * c <= n ? 0u : glob ^ (p & 1)) << (8 + p);
  }
  return m;
}
DI u32 tab_wait(rsrc_t r, u32 tag, int lane, int myv) {
  v2u t;
#pragma nounroll
  for (u32 sp_ = SPIN_CAP; sp_; --sp_) {
    asm volatile("" ::: "memory");
    t = ld8(r, OFF_TAB + myv * 1024 + (lane >> 3) * 128 + (lane & 7) * 8);
    if (__builtin_amdgcn_ballot_w64(t.y != tag) == 0) break;
    __builtin_amdgcn_s_sleep(4);
  }
  return tab_decode(t.x, lane);
}
DI float rcp(float v) { return __builtin_amdgcn_rcpf(v); }
DI f4 unpk(v4u a) { return f4{bf_lo(a.x), bf_hi(a.x), bf_lo(a.z), bf_hi(a.z)}; }
DI v4u tagged(f4 v, u32 tag) { return v4u{pk(v[0], v[1]), tag, pk(v[2], v[3]), tag}; }


DI int perm80(int m) {
  const int w = m / 80, r = m - 80 * w;
  return r < 64 ? 80 * w + (r & 32) + 8 * ((r >> 2) & 3) + 4 * ((r >> 4) & 1) + (r & 3) : m;
}
DI int swz16(int row) { return ((row & 7) << 1) | ((row >> 3) & 1); }
DI float silu_r(float v) { return rbf(v * __builtin_amdgcn_rcpf(1.f + __expf(-v))); }

DI v2u trread(LAS unsigned char* p) { return __builtin_bit_cast(v2u, __builtin_amdgcn_ds_read_tr16_b64_v4i16((LAS s4*)p)); }

DI f4 piece_reduce(f4 v, LAS unsigned char* red, int wpos, int lane, int w) {
  float a, b, c, d, e0, e1, f0;
  sw32(v[0], v[1], a, b); e0 = a + b;
  sw32(v[2], v[3], c, d); e1 = c + d;
  sw16(e0, e1, a, b); f0 = a + b;
  f0 += ror<4>(f0); f0 += ror<8>(f0);
  if ((lane & 12) == 0) *(LAS float*)(red + wpos) = f0;
  BAR();
  f4 r = {0.f, 0.f, 0.f, 0.f};
  if (w == 0) {
    r = *(LAS f4*)(red + (lane & 15) * 16) + *(LAS f4*)(red + 256 + (lane & 15) * 16);
    red48_4(r);
  }
  return r;
}

DI void mr_gather(rsrc_t rws, int m0, int bb, int blkA, int blkB, u32 tagv, f4& ra, f4& rb) {
  const int mo = OFF_MR + (m0 >> 6) * 1536 + bb * 256 + (m0 & 63) * 4;
  v4u a0, a1, a2, c0, c1, c2;
  POLL_BEGIN
  a0 = ld16(rws, mo + blkA * 15360); a1 = ld16(rws, mo + blkA * 15360 + 512); a2 = ld16(rws, mo + blkA * 15360 + 1024);
  c0 = ld16(rws, mo + blkB * 15360); c1 = ld16(rws, mo + blkB * 15360 + 512); c2 = ld16(rws, mo + blkB * 15360 + 1024);
  POLL_END(((ne4(a0, tagv) | ne4(a1, tagv) | ne4(a2, tagv)) | (ne4(c0, tagv) | ne4(c1, tagv) | ne4(c2, tagv))) != 0u)
  ra = unpk(a0) + unpk(a1) + unpk(a2);
  rb = unpk(c0) + unpk(c1) + unpk(c2);
}

__global__ void __launch_bounds__(512, 2)
fused_decode(const u16* __restrict__ P_X, const u16* __restrict__ P_CKV, const u16* __restrict__ P_WQ, const u16* __restrict__ P_WUK,
             const u16* __restrict__ P_WUV, const u16* __restrict__ P_WO, const u16* __restrict__ P_WG, const u16* __restrict__ P_WU,
             const u16* __restrict__ P_WD, const u16* __restrict__ P_C, const u16* __restrict__ P_WMOD, u16* __restrict__ P_OUT,
             unsigned char* __restrict__ P_WS) {
#define WSB ((unsigned char*)(((size_t)P_WS + 0x1fffff) & ~(size_t)0x1fffff))
#define KWAIT() asm volatile("" ::"s"(P_X), "s"(P_CKV), "s"(P_WQ), "s"(P_WUK), "s"(P_WUV), "s"(P_WO), "s"(P_WG), "s"(P_WU), "s"(P_WD), "s"(P_C), "s"(P_WMOD), "s"(P_OUT), "s"(P_WS))
  __shared__ __attribute__((aligned(16))) unsigned char smem[LDS_BYTES];
  LAS unsigned char* L = (LAS unsigned char*)smem;
  const u32 L0 = (u32)(size_t)L;
  const int tid = threadIdx.x, lane = tid & 63, w = UNI(tid >> 6);
  const int gidx = blockIdx.x, jj = gidx >> 3, idx = (gidx & 7) * 16 + (jj & 15);
  const int li = lane & 15, g = lane >> 4, q4 = (lane >> 2) & 3, p4 = lane & 3;
  {
    unsigned long long pc0;
    u32 clen;
    asm volatile("s_getpc_b64 %0\n.Lfd_pc:\n\ts_mov_b32 %1, .Lfd_end-.Lfd_pc\n\ts_max_i32 %1, %1, .Lfd_end2-.Lfd_pc" : "=s"(pc0), "=&s"(clen)::"scc");
    const u32 po = (u32)(((jj - 24) * 64 + lane) * 128);
    if (jj >= 24 && w == 7 && po < clen)
      asm volatile("s_mov_b32 m0, %2\n\ts_nop 0\n\tglobal_load_lds_dword %0, %1" ::"v"(po), "s"(pc0), "s"(L0 + L_SCR) : "memory");
  }

  const u32 myside = ((u32)__builtin_amdgcn_s_getreg(20 | (3 << 11)) >> 2) & 1u;
  const int myv = gidx & 7;
  if (__builtin_expect(jj >= 16, 1)) {
    const int xi = idx;
    u32 tagv;
    const int mi = (gidx & 7) * 8 + (jj & 7), hg = xi & 1;
    const int xc = xi >> 4, b = xc >> 2, chunk = (xc & 3) * 8 + ((xi & 15) >> 1);
    int cdone = 0;
#define CKV_ROWS(lo, hi) do { \
      const rsrc_t rck = mkrsrc(P_CKV, 2 * 4096 * 512 * 2); \
      _Pragma("unroll") for (int k = (lo); k < (hi); ++k) { \
        const int rl = (k + 8 * STGC(hg)) & 15, row = 16 * w + rl; \
        dmas(rck, (u32)((lane ^ swz16(rl)) * 16), (u32)((b * 4096 + chunk * 128 + row) * 1024), L0 + L_CK + row * 1024); \
      } } while (0)
    if (__builtin_expect(jj < 24 && mi < 60, 1)) {
      const int cg = mi / 6, ksp = mi - 6 * cg;
      u32 vm = (u32)((lane >> 3) * 7680 + ((lane & 7) ^ (g << 1)) * 16);
      const int rl = (w & 1) * 64 + lane, rg_ = 112 * ksp + rl;
      int oc = (w < 4 && rl < 112 && rg_ < 640) ? ((w >> 1) * 640 + rg_) * 2 : OOB;
      int spos = L_MS + (w >> 1) * 256 + (rl < 96 ? (rl & 96) + 8 * ((rl >> 2) & 3) + 4 * ((rl >> 4) & 1) + (rl & 3) : rl) * 2;
      int ox = ((li & 1) * 640 + 64 * cg + 16 * (w & 3) + 4 * g) * 2;
      int oxa = lane * 16, oxb = lane < 16 ? 1024 + lane * 16 : OOB;
      PIN(vm); PIN(oc); PIN(spos); PIN(ox); PIN(oxa); PIN(oxb);
      KWAIT();
      const rsrc_t rws = mkrsrc(WSB, WS_BYTES), rmod = mkrsrc(P_WMOD, 640 * 3840 * 2), rx = mkrsrc(P_X, 2560);
      u32 cv, ep; v2u xv;
      asm volatile("s_nop 4\n\tbuffer_load_ushort %0, %1, %2, 0 offen" : "=v"(cv) : "v"(oc), "s"(mkrsrc(P_C, 2560)) : "memory");
      asm volatile("s_nop 4\n\tbuffer_load_dwordx2 %0, %1, %2, 0 offen" : "=v"(xv) : "v"(ox), "s"(rx) : "memory");
      ld4sh(ep, rws, OFF_EPOCH);
      if (w < 7) {
        const u32 so_ = (u32)(((112 * ksp + 16 * w) * 3840 + 64 * cg) * 2);
        dma(rmod, vm, so_, L0 + L_MW + w * 2048);
        dma(rmod, vm, so_ + 1280, L0 + L_MW + 14336 + w * 2048);
        dma(rmod, vm, so_ + 8 * 7680, L0 + L_MW + w * 2048 + 1024);
        dma(rmod, vm, so_ + 8 * 7680 + 1280, L0 + L_MW + 14336 + w * 2048 + 1024);
        if (w < 4) {
          asm volatile("s_waitcnt vmcnt(6)" : "+v"(cv)::"memory");
          *(LAS u16*)(L + spos) = (u16)(__float_as_uint(silu_r(bf_lo(cv))) >> 16);
        } else if (mi * 3 + w - 4 < 128) {
          st16(rws, 2 * OFF_S2 + (mi * 3 + w - 4) * 2048 + lane * 16, v4u{SENT, SENT, SENT, SENT});
          st16(rws, 2 * OFF_S2 + (mi * 3 + w - 4) * 2048 + 1024 + lane * 16, v4u{SENT, SENT, SENT, SENT});
        }
      } else {
        const v4u x0 = __builtin_amdgcn_raw_buffer_load_b128(rx, oxa, 0, 0), x1 = __builtin_amdgcn_raw_buffer_load_b128(rx, oxb, 0, 0),
                  x2 = __builtin_amdgcn_raw_buffer_load_b128(rx, oxa + 1280, 0, 0), x3 = __builtin_amdgcn_raw_buffer_load_b128(rx, lane < 16 ? oxb + 1280 : OOB, 0, 0);
        float s0 = 0.f, q0 = 0.f, s1 = 0.f, q1 = 0.f;
#pragma unroll
        for (int k = 0; k < 4; ++k) {
          s0 = dot2(x0[k], 0x3f803f80u, s0); q0 = dot2(x0[k], x0[k], q0); s0 = dot2(x1[k], 0x3f803f80u, s0); q0 = dot2(x1[k], x1[k], q0);
          s1 = dot2(x2[k], 0x3f803f80u, s1); q1 = dot2(x2[k], x2[k], q1); s1 = dot2(x3[k], 0x3f803f80u, s1); q1 = dot2(x3[k], x3[k], q1);
        }
        float a0, a1, b0, b1, c0;
        sw32(s0, s1, a0, a1); a0 += a1;
        sw32(q0, q1, b0, b1); b0 += b1;
        sw16(a0, b0, a1, b1); c0 = a1 + b1;
        c0 += ror<1>(c0); c0 += ror<2>(c0); c0 += ror<4>(c0); c0 += ror<8>(c0);
        float sm, sq; sw16(c0, c0, sm, sq);
        const float mean = sm * (1.f / 640.f), var = fmaxf(sq * (1.f / 640.f) - mean * mean, 0.f);
        if ((lane & 31) == 0) *(LAS f2*)(L + L_XST + (lane >> 5) * 8) = f2{mean, __builtin_amdgcn_rsqf(var + 1e-5f)};
      }
      const int fq = (2 * g + (q4 >> 1)) & 3;
      int aoff = L_MW + (4 * g + q4) * 128 + (((2 * (w & 3)) ^ (fq << 1)) + (p4 >> 1)) * 16 + 8 * (p4 & 1);
      int boff = L_MS + (li & 1) * 256 + 16 * g, so1 = PH0(OFF_S1 + (li >> 1) * 30720 + mi * 512 + (w & 3) * 128 + (li & 1) * 64 + g * 16);
      PIN(aoff); PIN(boff); PIN(so1);
      if (w >= 4) {
        CKV_ROWS(0, EARLYC);
        asm volatile("s_waitcnt vmcnt(%c0)" ::"i"(EARLYC) : "memory");
      } else {
        VMWAIT(0);
      }
      asm volatile("; landed" : "+v"(xv), "+v"(ep) : "v"(cv) : "memory");
      tagv = ep + 1u;
      BAR();
      if (w < 4) {
        v2u ta[2][7];
#pragma unroll
        for (int k = 0; k < 2; ++k)
#pragma unroll
          for (int r = 0; r < 7; ++r) ta[k][r] = trread(L + aoff + k * 14336 + r * 2048);
        const v4u sb0 = *(LAS v4u*)(L + boff), sb1 = *(LAS v4u*)(L + boff + 64), sb2 = *(LAS v4u*)(L + boff + 128);
        const v2u sb3 = *(LAS v2u*)(L + boff + 192 - 8 * g);
        const f2 st = *(LAS f2*)(L + L_XST + (li & 1) * 8);
        __builtin_amdgcn_sched_barrier(0);
        f4 d[2];
#pragma unroll
        for (int k = 0; k < 2; ++k) {
          d[k] = mfma32(v4u{ta[k][0].x, ta[k][0].y, ta[k][1].x, ta[k][1].y}, sb0, f4{0.f, 0.f, 0.f, 0.f});
          d[k] = mfma32(v4u{ta[k][2].x, ta[k][2].y, ta[k][3].x, ta[k][3].y}, sb1, d[k]);
          d[k] = mfma32(v4u{ta[k][4].x, ta[k][4].y, ta[k][5].x, ta[k][5].y}, sb2, d[k]);
          d[k] = mfma32(v4u{ta[k][6].x, ta[k][6].y, 0u, 0u}, v4u{sb3.x, sb3.y, 0u, 0u}, d[k]);
        }
        const f4 xn = (f4{bf_lo(xv.x), bf_hi(xv.x), bf_lo(xv.y), bf_hi(xv.y)} - st.x) * st.y;
        const float one = ksp == 0 ? 1.f : 0.f;
        const v4u hv = tagged(xn * (d[1] + one) + d[0], tagv);
        st16(rws, so1, hv);
        st16(rws, so1 + 8192, hv);
      }
      BAR();
      if (w >= 4) { CKV_ROWS(EARLYC, 16); cdone = 1; }
    } else {
      u32 v8 = (u32)((lane >> 3) * 8192 + (lane & 7) * 16), v1 = (u32)lane * 16;
      PIN(v8); PIN(v1);
      KWAIT();
      if (jj >= 32 - TLBW && w == 6) {
        const int bi = lane >> 3, mb = lane & 7;
        const unsigned long long base = bi == 0 ? (unsigned long long)P_WUV : bi == 1 ? (unsigned long long)P_WO : bi == 2 ? (unsigned long long)P_WG : bi == 3 ? (unsigned long long)P_WU
                                      : bi == 4 ? (unsigned long long)P_WD : bi == 5 ? (unsigned long long)P_CKV : bi == 6 ? (unsigned long long)P_WUK : (unsigned long long)P_OUT;
        const u32 size = bi == 1 ? 5242880u : (bi >= 2 && bi <= 4) ? 2211840u : bi == 5 ? 8388608u : bi == 7 ? 2560u : 4194304u;
        u32 off = (u32)mb << 20;
        off = off < size ? off : size - 4u;
        const unsigned long long pa = base + off;
        asm volatile("s_mov_b32 m0, %1\n\ts_nop 0\n\tglobal_load_lds_dword %0, off" ::"v"(pa), "s"(L0 + L_SCR) : "memory");
      }
      u32 nearbit = 0, ep0;
      ld4sh(ep0, mkrsrc(WSB, WS_BYTES), OFF_EPOCH);
      if (jj == 24) {
        const rsrc_t rp = mkrsrc(WSB, WS_BYTES);
        const int pg = (myv + w) % 5;
        const int pa = lane < 8 ? (pg << 21) + (pg == 0 ? 2015232 : 1572864 + myv * 16384) + (pg == 0 ? myv * 128 : 0) + w * 1024 + lane * 16 : OOB;
        const v4u pv = {0u, 0u, 0u, 0u};
        unsigned long long t0, t1, t2, t3;
        st16(rp, pa, pv); st16(rp, pa + 8192, pv);
        VMWAIT(0);
        st16(rp, pa, pv); st16(rp, pa + 8192, pv);
        asm volatile("s_waitcnt vmcnt(1)\n\ts_memtime %0\n\ts_waitcnt vmcnt(0)\n\ts_memtime %1\n\ts_waitcnt lgkmcnt(0)" : "=&s"(t0), "=&s"(t1)::"memory");
        st16(rp, pa + 8192, pv); st16(rp, pa, pv);
        asm volatile("s_waitcnt vmcnt(1)\n\ts_memtime %0\n\ts_waitcnt vmcnt(0)\n\ts_memtime %1\n\ts_waitcnt lgkmcnt(0)" : "=&s"(t2), "=&s"(t3)::"memory");
        nearbit = (u32)(t1 - t0) > (u32)(t3 - t2) ? 0u : 1u;
      }
      asm volatile("s_waitcnt vmcnt(0)" : "+v"(ep0)::"memory");
      tagv = ep0 + 1u;
      if (jj == 24) {
        const rsrc_t rp = mkrsrc(WSB, WS_BYTES);
        if (lane == 0) ((LAS u32*)(L + L_XST))[w] = myside | ((nearbit ^ myside) << 1);
        BAR();
        if (w == 0) {
          const v2u two = *(LAS v2u*)(L + L_XST + (lane & 3) * 8);
          st16(rp, OFF_TAB + (lane >> 3) * 1024 + myv * 128 + (lane & 7) * 16, v4u{two.x, tagv, two.y, tagv});
        }
        BAR();
      }
      if (jj > 24) {
        const int hl = jj - 24, slot = (3 - (int)((size_t)P_WQ >> 7)) & 7;
        const rsrc_t rq = mkrsrc(P_WQ, 640 * 4096 * 2), rk = mkrsrc(P_WUK, 512 * 4096 * 2);
        dmah(rq, v8, (u32)((80 * hl + 8 * w) * 8192 + (gidx & 7) * 1024 + slot * 128), L0 + L_QI + w * 1024);
        if (w < 2) dmah(rq, v8, (u32)((80 * hl + 64 + 8 * w) * 8192 + (gidx & 7) * 1024 + slot * 128), L0 + L_QI + w * 1024);
        else if (w - 2 + 6 * (hl - 1) < 10) dmah(rq, v8, (u32)((8 * (w - 2 + 6 * (hl - 1))) * 8192 + (gidx & 7) * 1024 + slot * 128), L0 + L_QI + w * 1024);
        {
          const int u = (hl - 1) * 8 + w;
          const u32 ck = (u32)((gidx & 7) * 1024 + ((3 - (int)((size_t)P_WUK >> 7)) & 7) * 128);
          dmah(rk, v8, (u32)(u * 65536) + ck, L0 + L_QI + w * 1024);
          if (u >= 48) dmah(rk, v8, (u32)((8 + u) * 65536) + ck, L0 + L_QI + w * 1024);
        }
      }
      BAR();
      if (jj != 24) for (int i = 0; i < XDELAY; ++i) __builtin_amdgcn_s_sleep(4);
    }
    const rsrc_t rws = mkrsrc(WSB, WS_BYTES);
    if (cdone == 0) CKV_ROWS(0, 16);
    BAR();
    v2u tb, s1t;
    {
      const int ot = OFF_TAB + myv * 1024 + (lane >> 3) * 128 + (lane & 7) * 8, os = w == 0 ? PH0(OFF_S1 + myv * 30720 + (lane < 60 ? lane : 0) * 512) : OOB;
      asm volatile("s_nop 4\n\tbuffer_load_dwordx2 %0, %1, %2, 0 offen sc1" : "=v"(tb) : "v"(ot), "s"(rws) : "memory");
      asm volatile("buffer_load_dwordx2 %0, %1, %2, 0 offen sc1" : "=v"(s1t) : "v"(os), "s"(rws) : "memory");
    }
    v4u ca_[16], ct_[4][4];
    int qoff[8], qw, epos = L_EI + (lane * 8 + w) * 4, ppos = L_PI + ((w >> 1) * 64 + lane) * 16 + (w & 1) * 8, cto[4];
    qw = L_QI + (li * 64 + g * 16 + (w ^ (li & 7))) * 16;
    PIN(epos); PIN(ppos); PIN(qw);
#pragma unroll
    for (int s = 0; s < 8; ++s) { qoff[s] = L_QI + (s * 64 + g * 16 + (li ^ s)) * 16; PIN(qoff[s]); }
#pragma unroll
    for (int ct = 0; ct < 4; ++ct) {
      const int ch = 8 * w + 2 * ct + (p4 >> 1);
      cto[ct] = L_CK + (4 * g + q4) * 1024 + ((ch & ~15) | ((ch & 15) ^ swz16(4 * g + q4))) * 16 + 8 * (p4 & 1);
      PIN(cto[ct]);
    }
    u32 vgu = (u32)((lane >> 1) * 3456 + (lane & 1) * 16), vgu2 = lane >= 32 ? (u32)OOB : vgu, vdn[3];
#pragma unroll
    for (int k = 0; k < 3; ++k) {
      const int P = 64 * (w + 8 * k) + lane, row = P / 80, pos = P - 80 * row;
      vdn[k] = (u32)(row * 1280 + ((pos & ~15) | ((pos & 15) ^ swz16(row))) * 16);
      PIN(vdn[k]);
    }
    PIN(vgu); PIN(vgu2);
    VMWAIT(2);
    {
      const int fi = swz16(li);
#pragma unroll
      for (int s = 0; s < 16; ++s) ca_[s] = *(LAS v4u*)(L + L_CK + (16 * w + li) * 1024 + ((s ^ fi) + 16 * g) * 16);
    }
    asm volatile("s_waitcnt vmcnt(0)" : "+v"(tb), "+v"(s1t)::"memory");
    const u32 smask = __builtin_amdgcn_ballot_w64(tb.y != tagv) == 0 ? tab_decode(tb.x, lane) : tab_wait(rws, tagv, lane, myv);
    int so3 = PH(OFF_S3 + xi * 16384 + (li & 7) * 1024 + w * 128 + (li >> 3) * 64 + g * 16, hg * 4 + ((li & 7) >> 2), smask);
    int so3b = PH(OFF_S3 + xi * 16384 + 8192 + (li & 7) * 1024 + w * 128 + (li >> 3) * 64 + g * 16, hg * 4 + 2 + ((li & 7) >> 2), smask);
    int sf3 = (li >> 3) == (g >> 1) ? PH(OFF_S3F + g * 131072 + (xi * 8 + w) * 128 + (li & 7) * 16, hg * 4 + g, smask) : OOB;
    PIN(so3); PIN(so3b); PIN(sf3);
    int o0 = PHS(OFF_S2 + ((b * 2) * 32 + hg * 16) * 1024 + tid * 16, myside, smask), og2 = (lane & 15) == 0 ? o0 + (g & 1) * 65536 : OOB;
    PIN(o0); PIN(og2);
    if (w == 0 && __builtin_amdgcn_ballot_w64(s1t.y != tagv) != 0) {
      const int o = PH0(OFF_S1 + myv * 30720 + (lane < 60 ? lane : 0) * 512);
      POLL_BEGIN
      const v2u t = ld8(rws, o);
      if (__builtin_amdgcn_ballot_w64(t.y != tagv) == 0) break;
      __builtin_amdgcn_s_sleep(4); }
    }
    BAR();
    u32 look;
    ld4sh(look, rws, og2);
#pragma unroll
    for (int ct = 0; ct < 4; ++ct)
#pragma unroll
      for (int s = 0; s < 4; ++s) {
        const v2u lo = trread(L + cto[ct] + (32 * s) * 1024), hi = trread(L + cto[ct] + (32 * s + 16) * 1024);
        ct_[ct][s] = v4u{lo.x, lo.y, hi.x, hi.y};
      }
#define GD_PIECES(lo, hi) do { \
      const rsrc_t rg = mkrsrc(P_WG, 640 * 1728 * 2), ru = mkrsrc(P_WU, 640 * 1728 * 2); \
      const int r0 = STG((3 * (xi & 3)) >> 1); \
      _Pragma("unroll") for (int i = (lo); i < (hi); ++i) { \
        int pc = r0 + i; pc = pc >= 6 ? pc - 6 : pc; \
        const int mt = pc >= 3 ? 1 : 0, k = pc - 3 * mt; \
        const u32 so_ = (u32)(((80 * w + 32 * k) * 1728 + 16 * xi) * 2), ld_ = L0 + L_GU + w * 6144 + pc * 1024; \
        if (mt) dmas(ru, k == 2 ? vgu2 : vgu, so_, ld_); else dmas(rg, k == 2 ? vgu2 : vgu, so_, ld_); \
      } } while (0)
    {
      const int o1_ = o0 + 16384, o2_ = o0 + 65536, o3_ = o0 + 81920;
      v4u a0, a1, b0, b1;
      asm volatile("s_waitcnt vmcnt(0)" : "+v"(look)::"memory");
      if (__builtin_amdgcn_ballot_w64((lane & 15) == 0 && look != SENT) == 0) gate<false>(rws, og2, 0u, lane);
      POLL_BEGIN
      a0 = ld16(rws, o0); a1 = ld16(rws, o1_); b0 = ld16(rws, o2_); b1 = ld16(rws, o3_);
      POLL_END(umax(umax(umax4(a0), umax4(a1)), umax(umax4(b0), umax4(b1))) == SENT)
      v4u r0, r1;
#pragma unroll
      for (int k = 0; k < 4; ++k) {
        r0[k] = pk(bf_lo(a0[k]) + bf_lo(b0[k]), bf_hi(a0[k]) + bf_hi(b0[k]));
        r1[k] = pk(bf_lo(a1[k]) + bf_lo(b1[k]), bf_hi(a1[k]) + bf_hi(b1[k]));
      }
      *(LAS v4u*)(L + qw) = r0;
      *(LAS v4u*)(L + qw + 128) = r1;
      st16(rws, so3, v4u{SENT, SENT, SENT, SENT});
      st16(rws, so3b, v4u{SENT, SENT, SENT, SENT});
    }
    BAR();
    {
      f4 sc = {0.f, 0.f, 0.f, 0.f};
      v4u qf[16];
#pragma unroll
      for (int s = 0; s < 16; ++s) qf[s] = *(LAS v4u*)(L + qoff[s & 7] + (s >> 3) * 8192);
      __builtin_amdgcn_sched_barrier(0);
      {
        const rsrc_t rd = mkrsrc(P_WD, 1728 * 640 * 2);
        const u32 vd2 = w < 4 ? vdn[2] : (u32)OOB;
#pragma unroll
        for (int s = 0; s < 16; ++s) {
          sc = mfma32(ca_[(s >> 1) + 8 * (s & 1)], qf[(s >> 1) + 8 * (s & 1)], sc);
          if (s == 5 || s == 9 || s == 13) {
            __builtin_amdgcn_sched_barrier(0);
            dma(rd, s == 5 ? vdn[0] : s == 9 ? vdn[1] : vd2, (u32)(16 * xi * 1280), L0 + L_WD + (w + 2 * (s - 5)) * 1024);
            __builtin_amdgcn_sched_barrier(0);
          }
        }
      }
      const float m01 = __builtin_amdgcn_fmed3f(sc[0], sc[1], 3.0e38f);
      const float E = ceilf(max32v(max16v(vmax3(m01, sc[2], sc[3]))));
      const float p0 = __builtin_amdgcn_exp2f(sc[0] - E), p1 = __builtin_amdgcn_exp2f(sc[1] - E), p2 = __builtin_amdgcn_exp2f(sc[2] - E), p3 = __builtin_amdgcn_exp2f(sc[3] - E);
      if (lane < 16) *(LAS float*)(L + epos) = E * 128.f;
      *(LAS v2u*)(L + ppos) = v2u{pk(p0, p1), pk(p2, p3)};
      BAR();
      const f4 e0 = *(LAS f4*)(L + L_EI + li * 32), e1 = *(LAS f4*)(L + L_EI + li * 32 + 16);
      v4u pf[4];
#pragma unroll
      for (int s = 0; s < 4; ++s) pf[s] = *(LAS v4u*)(L + L_PI + (s * 64 + lane) * 16);
      __builtin_amdgcn_sched_barrier(0);
      const float Em = vmax(vmax3(e0[0], e0[1], e0[2]), vmax3(vmax3(e0[3], e1[0], e1[1]), e1[2], e1[3]));
      f4 o[4];
#pragma unroll
      for (int ct = 0; ct < 4; ++ct) o[ct] = f4{0.f, 0.f, 0.f, 0.f};
      float lt = 0.f;
#pragma unroll
      for (int s = 0; s < 4; ++s) {
        const float ea = s < 2 ? e0[2 * s] : e1[2 * s - 4], eb = s < 2 ? e0[2 * s + 1] : e1[2 * s - 3];
        const u32 ua = (u32)fminf(Em - ea, 32640.f), ub = (u32)fminf(Em - eb, 32640.f);
        const v4u pp = subsat4(pf[s], ua, ub);
#pragma unroll
        for (int ct = 0; ct < 4; ++ct) o[ct] = mfma32(ct_[ct][s], pp, o[ct]);
#pragma unroll
        for (int k = 0; k < 4; ++k) lt = dot2(pp[k], 0x3f803f80u, lt);
      }
      lt = sum32x(sum16x(lt));
      const float inv = rcp(lt);
      VMWAIT(3);
      {
        const u32 lam = __float_as_uint(Em * (1.f / 128.f) + __builtin_amdgcn_logf(lt));
        st16(rws, sf3, v4u{lam, tagv, lam, tagv});
      }
      {
        const f4 x0 = o[0] * inv, x1 = o[1] * inv, x2 = o[2] * inv, x3 = o[3] * inv;
        const v4u u0 = {pk(x0[0], x0[1]), pk(x0[2], x0[3]), pk(x1[0], x1[1]), pk(x1[2], x1[3])};
        const v4u u1 = {pk(x2[0], x2[1]), pk(x2[2], x2[3]), pk(x3[0], x3[1]), pk(x3[2], x3[3])};
        v4u sa, sb;
#pragma unroll
        for (int k = 0; k < 4; ++k) {
          sa[k] = (u32)__builtin_amdgcn_update_dpp((int)u0[k], (int)u1[k], 0x128, 0xf, 0xc, false);
          sb[k] = (u32)__builtin_amdgcn_update_dpp((int)u1[k], (int)u0[k], 0x128, 0xf, 0x3, false);
        }
        st16(rws, so3, nosent4(sa));
        st16(rws, so3b, nosent4(sb));
      }
    }
    BAR();
    if (xi < 108) {
      GD_PIECES(GD_EARLY, 6);
      BAR();
      const int bq = lane & 1, m0 = 80 * w + 4 * (lane < 40 ? lane >> 1 : 0);
      f4 sb4, cb4;
      mr_gather(rws, m0, bq, 1, 2, tagv, sb4, cb4);
#pragma unroll
      for (int i = 0; i < 4; ++i) cb4[i] = rbf(rbf(cb4[i]) + 1.f);
      int hpos = L_H2 + bq * 1280 + perm80(m0) * 2;
      int o5 = PH(OFF_S5 + (gidx & 7) * 10240 + (m0 >> 3) * 128 + bq * 32 + ((m0 >> 2) & 1) * 16, myv, smask);
      int gpos = L_GRED + (g * 16 + 2 * w + (li & 1)) * 16;
      PIN(cb4); PIN(hpos); PIN(o5); PIN(gpos);
      if (lane < 40) {
        *(LAS v2u*)(L + hpos) = v2u{pk(cb4[0], cb4[1]), pk(cb4[2], cb4[3])};
        *(LAS v2u*)(L + hpos + 2560) = v2u{pk(sb4[0], sb4[1]), pk(sb4[2], sb4[3])};
      }
      VMWAIT(0);
      LGKM0();
      v4u gA[2][3]; v2u da_[5];
      f4 g1, u1, g2, u2;
      {
        LAS unsigned char* base = L + L_GU + w * 6144 + (4 * g + q4) * 32 + 8 * p4;
#pragma unroll
        for (int m = 0; m < 2; ++m) {
          const v2u t0 = trread(base + m * 3072), t1 = trread(base + m * 3072 + 512), t2 = trread(base + m * 3072 + 1024), t3 = trread(base + m * 3072 + 1536),
                    t4 = trread(base + m * 3072 + 2048);
          gA[m][0] = v4u{t0.x, t0.y, t1.x, t1.y}; gA[m][1] = v4u{t2.x, t2.y, t3.x, t3.y}; gA[m][2] = v4u{t4.x, t4.y, 0u, 0u};
        }
        LAS unsigned char* hb = L + L_H2 + (li & 3) * 1280 + 160 * w;
        const v4u h0 = *(LAS v4u*)(hb + 16 * g), h1 = *(LAS v4u*)(hb + 64 + 16 * g);
        const v2u h2 = *(LAS v2u*)(hb + 128 + 8 * g);
        f4 a[2];
#pragma unroll
        for (int m = 0; m < 2; ++m) {
          a[m] = mfma32(gA[m][0], h0, f4{0.f, 0.f, 0.f, 0.f});
          a[m] = mfma32(gA[m][1], h1, a[m]);
          a[m] = mfma32(gA[m][2], v4u{h2.x, h2.y, 0u, 0u}, a[m]);
        }
        if (li < 4) {
          *(LAS f4*)(L + gpos + 4096 + (li >> 1) * 2048) = a[0];
          *(LAS f4*)(L + gpos + 4096 + (li >> 1) * 2048 + 1024) = a[1];
        }
        BAR();
        g1 = *(LAS f4*)(L + L_GRED + 4096 + lane * 16); u1 = *(LAS f4*)(L + L_GRED + 5120 + lane * 16);
        g2 = *(LAS f4*)(L + L_GRED + 6144 + lane * 16); u2 = *(LAS f4*)(L + L_GRED + 7168 + lane * 16);
        const int row = 4 * g + q4, fr = swz16(row);
#pragma unroll
        for (int t = 0; t < 5; ++t) {
          const int ch = 2 * (5 * w + t) + (p4 >> 1);
          da_[t] = trread(L + L_WD + row * 1280 + ((ch & ~15) | ((ch & 15) ^ fr)) * 16 + 8 * (p4 & 1));
        }
        red248_8(g1, u1);
        red248_8(g2, u2);
      }
      int so = li < 10 ? PH(OFF_S6 + xi * 5120 + (5 * w + (li >> 1)) * 128 + (g >> 1) * 64 + (li & 1) * 32 + (g & 1) * 16, (5 * w + (li >> 1)) >> 3, smask) : OOB;
      u32 tm[5];
#pragma unroll
      for (int t = 0; t < 5; ++t) { tm[t] = (li >> 1) == t ? 0xffffffffu : 0u; PIN(tm[t]); }
      int hbo = L_H2 + (li & 1) * 1280 + 160 * w + 16 * g, spos = L_GRED + 2048 + (2 * w + (lane & 1)) * 8 + (lane >> 5) * 4;
      PIN(so); PIN(g1); PIN(u1); PIN(g2); PIN(u2); PIN(hbo); PIN(spos);
      {
        v4u a;
        POLL_BEGIN
        a = ld16(rws, o5);
        POLL_END(ne4(a, tagv) != 0u)
        const f4 vin = unpk(a) * cb4;
        if (lane < 40) *(LAS v2u*)(L + hpos) = v2u{pk(vin[0], vin[1]), pk(vin[2], vin[3])};
        LGKM0();
        const v4u h0 = *(LAS v4u*)(L + hbo), h1 = *(LAS v4u*)(L + hbo + 64);
        const v2u h2 = *(LAS v2u*)(L + hbo + 128 - 8 * g);
        __builtin_amdgcn_sched_barrier(0);
        const u32 xa = lane < 40 ? a.x : 0u, xb = lane < 40 ? a.z : 0u;
        float a0, a1;
        sw32(dot2(xb, 0x3f803f80u, dot2(xa, 0x3f803f80u, 0.f)), dot2(xb, xb, dot2(xa, xa, 0.f)), a0, a1);
        a0 += a1;
        a0 += ror<2>(a0); a0 += ror<4>(a0); a0 += ror<8>(a0); a0 = sum16x(a0);
        f4 ac[2];
#pragma unroll
        for (int m = 0; m < 2; ++m) {
          ac[m] = mfma32(gA[m][0], h0, f4{0.f, 0.f, 0.f, 0.f});
          ac[m] = mfma32(gA[m][1], h1, ac[m]);
          ac[m] = mfma32(gA[m][2], v4u{h2.x, h2.y, 0u, 0u}, ac[m]);
        }
        if ((lane & 30) == 0) *(LAS float*)(L + spos) = a0;
        if (li < 2) {
          *(LAS f4*)(L + gpos) = ac[0];
          *(LAS f4*)(L + gpos + 1024) = ac[1];
        }
        BAR();
        f2 st = *(LAS f2*)(L + L_GRED + 2048 + li * 8);
        f4 gg = *(LAS f4*)(L + L_GRED + lane * 16), uu = *(LAS f4*)(L + L_GRED + (64 + lane) * 16);
        red248_10(gg, uu, st);
        const float mean = st.x * (1.f / 640.f), var = fmaxf(st.y * (1.f / 640.f) - mean * mean, 0.f);
        const float rs = __builtin_amdgcn_rsqf(var + 1e-5f);
        float hh[4];
#pragma unroll
        for (int i = 0; i < 4; ++i) {
          const float gv = rs * (gg[i] - mean * g1[i]) + g2[i], uv = rs * (uu[i] - mean * u1[i]) + u2[i];
          hh[i] = uv * rcp(1.f + __expf(-gv));
        }
        const v2u hb = {pk(hh[0], hh[1]), pk(hh[2], hh[3])};
        f4 o = {0.f, 0.f, 0.f, 0.f};
#pragma unroll
        for (int t = 0; t < 5; ++t) o = mfma16(da_[t], v2u{hb.x & tm[t], hb.y & tm[t]}, o);
        st16(rws, so, tagged(o, tagv));
      }
    }
  } else {
    const int yi = idx, h = yi >> 2, sub = yi & 3, dh = sub & 1, ch = sub >> 1;
    u32 vq = (u32)((lane >> 3) * 8192 + ((lane & 7) ^ (g << 1)) * 16);
    PIN(vq);
    KWAIT();
    const rsrc_t rws = mkrsrc(WSB, WS_BYTES);
    u32 ep; v2u tb;
    for (int i = 0; i < YDELAY; ++i) __builtin_amdgcn_s_sleep(2);
    {
      const rsrc_t rq = mkrsrc(P_WQ, 640 * 4096 * 2), rk = mkrsrc(P_WUK, 512 * 4096 * 2);
      const int k0 = 5 * STG(ch), k1 = 5 - k0;
#pragma unroll
      for (int k = 0; k < 5; ++k) dmas(rq, vq, (u32)(((80 * w + 8 * (k0 + k)) * 4096 + h * 128 + dh * 64) * 2), L0 + L_WQ + w * 10240 + (k0 + k) * 1024);
      ld4sh(ep, rws, OFF_EPOCH);
#pragma unroll
      for (int k = 0; k < 5; ++k) dmas(rq, vq, (u32)(((80 * w + 8 * (k1 + k)) * 4096 + h * 128 + dh * 64) * 2), L0 + L_WQ + w * 10240 + (k1 + k) * 1024);
#pragma unroll
      for (int k = 0; k < 4; ++k) {
        const int ii = 8 * (k & 1) + (lane >> 3), cl = 8 * (ii >> 2) + 4 * (k >> 1) + (ii & 3);
        dmas(rk, (u32)(cl * 8192 + ((lane & 7) ^ ((ii >> 1) & 7)) * 16), (u32)(((ch * 256 + 32 * w) * 4096 + h * 128 + dh * 64) * 2), L0 + L_WK + w * 4096 + k * 1024);
      }
    }
    const int bq = (lane >= 20 && lane < 40) ? 1 : 0, m0 = 80 * w + 4 * (lane < 40 ? lane - 20 * bq : 0);
    const int o1l = OFF_S1 + (gidx & 7) * 30720 + 6 * (m0 >> 6) * 512 + ((m0 >> 4) & 3) * 128 + bq * 64 + (m0 & 15) * 4;
    int o1[6];
#pragma unroll
    for (int k = 0; k < 6; ++k) { o1[k] = PH0(o1l + 512 * k); PIN(o1[k]); }
    int hpos = L_H + bq * 1280 + perm80(m0) * 2;
    int so2 = li < 4 ? PH0(OFF_S2 + ((((li & 1) * 2 + dh) * 32 + h) * 512 + ch * 256 + 32 * w + 8 * g) * 2) + (li >> 1) * 8192 : OOB;
    PIN(hpos); PIN(so2);
    u32 pgo = (u32)(((lane >> 3) + (lane & 7)) % 5) & 1u;
    PIN(pgo);
    u32 vo[5];
#pragma unroll
    for (int k = 0; k < 5; ++k) {
      const int P = 64 * (5 * w + k) + lane, row = P / 80, pos = P - 80 * row;
      vo[k] = (u32)(row * 1280 + ((pos & ~15) | ((pos & 15) ^ swz16(row & 15))) * 16);
      PIN(vo[k]);
    }
    BAR();
      {
        const int ot = OFF_TAB + myv * 1024 + (lane >> 3) * 128 + (lane & 7) * 8;
        asm volatile("s_nop 4\n\tbuffer_load_dwordx2 %0, %1, %2, 0 offen sc1" : "=v"(tb) : "v"(ot), "s"(rws) : "memory");
      }
    asm volatile("s_waitcnt vmcnt(5)" : "+v"(ep)::"memory");
    const u32 tagv = ep + 1u;
    const int fq = (2 * g + (q4 >> 1)) & 3;
    {
      v4u qa[4][3], ka_[2][2];
#pragma unroll
      for (int nt = 0; nt < 4; ++nt) {
        LAS unsigned char* base = L + L_WQ + w * 10240 + (4 * g + q4) * 128 + (((2 * nt) ^ (fq << 1)) + (p4 >> 1)) * 16 + 8 * (p4 & 1);
        const v2u t0 = trread(base), t1 = trread(base + 2048), t2 = trread(base + 4096), t3 = trread(base + 6144), t4 = trread(base + 8192);
        qa[nt][0] = v4u{t0.x, t0.y, t1.x, t1.y}; qa[nt][1] = v4u{t2.x, t2.y, t3.x, t3.y}; qa[nt][2] = v4u{t4.x, t4.y, 0u, 0u};
      }
      asm volatile("s_waitcnt vmcnt(0)" : "+v"(tb)::"memory");
      {
        const unsigned long long in = __builtin_amdgcn_ballot_w64(tb.y == tagv);
        const unsigned long long vb = __builtin_amdgcn_ballot_w64(((tb.x >> 1) & 1u) != pgo) & in;
        const int hsel = (int)(((2 * __builtin_popcountll(vb) > __builtin_popcountll(in) ? 1u : 0u) ^ myside) << 13);
#pragma unroll
        for (int k = 0; k < 6; ++k) o1[k] += hsel;
      }
      v4u a0 = ld16(rws, o1[0]), a1 = ld16(rws, o1[1]), a2 = ld16(rws, o1[2]), a3 = ld16(rws, o1[3]), a4 = ld16(rws, o1[4]), a5 = ld16(rws, o1[5]);
      const int fk = (li >> 1) & 7;
#pragma unroll
      for (int s = 0; s < 2; ++s) {
        LAS unsigned char* b0 = L + L_WK + w * 4096 + li * 128 + ((4 * s + (g >> 1)) ^ fk) * 16 + 8 * (g & 1);
        LAS unsigned char* b1 = L + L_WK + w * 4096 + li * 128 + ((4 * s + 2 + (g >> 1)) ^ fk) * 16 + 8 * (g & 1);
#pragma unroll
        for (int t = 0; t < 2; ++t) { const v2u lo = *(LAS v2u*)(b0 + t * 2048), hi = *(LAS v2u*)(b1 + t * 2048); ka_[t][s] = v4u{lo.x, lo.y, hi.x, hi.y}; }
      }
#pragma nounroll
      for (u32 sp_ = SPIN_CAP; sp_; --sp_) {
        const u32 bad = (ne4(a0, tagv) | ne4(a1, tagv) | ne4(a2, tagv)) | (ne4(a3, tagv) | ne4(a4, tagv) | ne4(a5, tagv));
        if (__builtin_amdgcn_ballot_w64(bad != 0u) == 0) break;
        asm volatile("" ::: "memory");
        __builtin_amdgcn_s_sleep(S1SLEEP);
        a0 = ld16(rws, o1[0]); a1 = ld16(rws, o1[1]); a2 = ld16(rws, o1[2]); a3 = ld16(rws, o1[3]); a4 = ld16(rws, o1[4]); a5 = ld16(rws, o1[5]);
      }
      CWAIT();
      const f4 hs = (unpk(a0) + unpk(a1)) + (unpk(a2) + unpk(a3)) + (unpk(a4) + unpk(a5));
      if (lane < 40) *(LAS v2u*)(L + hpos) = v2u{pk(hs[0], hs[1]), pk(hs[2], hs[3])};
      LGKM0();
      LAS unsigned char* hb = L + L_H + (li & 1) * 1280 + 160 * w;
      const v4u h0 = *(LAS v4u*)(hb + 16 * g), h1 = *(LAS v4u*)(hb + 64 + 16 * g);
      const v2u h2 = *(LAS v2u*)(hb + 128 + 8 * g);
      f4 a[4];
#pragma unroll
      for (int nt = 0; nt < 4; ++nt) {
        a[nt] = mfma32(qa[nt][0], h0, f4{0.f, 0.f, 0.f, 0.f});
        a[nt] = mfma32(qa[nt][1], h1, a[nt]);
        a[nt] = mfma32(qa[nt][2], v4u{h2.x, h2.y, 0u, 0u}, a[nt]);
      }
      __builtin_amdgcn_sched_barrier(0);
      {
        const rsrc_t rv = mkrsrc(P_WUV, 512 * 4096 * 2);
#pragma unroll
        for (int k = 0; k < 4; ++k) dma(rv, vq, (u32)(((ch * 256 + 8 * (4 * w + k)) * 4096 + h * 128 + dh * 64) * 2), L0 + L_WV + (4 * w + k) * 1024);
      }
      __builtin_amdgcn_sched_barrier(0);
      if (li < 2) {
#pragma unroll
        for (int s = 0; s < 2; ++s)
          *(LAS v4u*)(L + L_QP + (s * 64 + g * 16 + 2 * w + li) * 16) =
              v4u{pk(a[2 * s][0], a[2 * s][1]), pk(a[2 * s][2], a[2 * s][3]), pk(a[2 * s + 1][0], a[2 * s + 1][1]), pk(a[2 * s + 1][2], a[2 * s + 1][3])};
      }
      BAR();
      const v4u q0 = *(LAS v4u*)(L + L_QP + lane * 16), q1 = *(LAS v4u*)(L + L_QP + (64 + lane) * 16);
      f4 d[2];
#pragma unroll
      for (int t = 0; t < 2; ++t) {
        d[t] = mfma32(ka_[t][0], q0, f4{0.f, 0.f, 0.f, 0.f});
        d[t] = mfma32(ka_[t][1], q1, d[t]);
      }
      constexpr float SC = 0.08838834764831845f * 1.4426950408889634f;
      d[0] *= SC; d[1] *= SC;
      red248_8(d[0], d[1]);
      {
        v4u qv = nosent4(v4u{pk(d[0][0], d[0][1]), pk(d[0][2], d[0][3]), pk(d[1][0], d[1][1]), pk(d[1][2], d[1][3])});
#pragma unroll
        for (int k = 0; k < 4; ++k) qv[k] = (u32)__builtin_amdgcn_update_dpp(0, (int)qv[k], 0x44, 0xf, 0xf, false);
        st16(rws, so2, qv);
      }
    }
    BAR();
    v4u va[8], oa[2][5];
    u32 smask;
    int oa_[4], ol[4];
    v4u l0, l1, l2, l3;
    {
      const bool mr = yi < 120;
      const int it = yi / 3, kth = yi - 3 * it, blk = it / 10, cg = it - 10 * blk;
      const int rl = (w & 3) * 64 + lane, rg_ = 224 * kth + rl;
      u32 cv;
      {
        const int oc = (rl < 224 && rg_ < 640) ? ((w >> 2) * 640 + rg_) * 2 : OOB;
        asm volatile("s_nop 4\n\tbuffer_load_ushort %0, %1, %2, 0 offen" : "=v"(cv) : "v"(oc), "s"(mkrsrc(P_C, 2560)) : "memory");
      }
      const rsrc_t ro = mkrsrc(P_WO, 4096 * 640 * 2);
#pragma unroll
      for (int k = 0; k < 5; ++k) dmas(ro, vo[k], (u32)((h * 128 + dh * 64 + 32 * STG(ch)) * 1280), L0 + L_WO + STG(ch) * 40960 + (5 * w + k) * 1024);
      if (mr && w < 7) {
        const rsrc_t rmod = mkrsrc(P_WMOD, 640 * 3840 * 2);
        const u32 vm = (u32)((lane >> 3) * 7680 + ((lane & 7) ^ (g << 1)) * 16);
#pragma unroll
        for (int k = 0; k < 4; ++k) dma(rmod, vm, (u32)(((224 * kth + 32 * w + 8 * k) * 3840 + (2 + blk) * 640 + 64 * cg) * 2), L0 + L_YMW + (4 * w + k) * 1024);
      }
#pragma unroll
      for (int k = 0; k < 5; ++k) dmas(ro, vo[k], (u32)((h * 128 + dh * 64 + 32 * (STG(ch) ^ 1)) * 1280), L0 + L_WO + (STG(ch) ^ 1) * 40960 + (5 * w + k) * 1024);
      asm volatile("s_waitcnt vmcnt(10)" : "+v"(cv)::"memory");
      *(LAS u16*)(L + L_YMS + (w >> 2) * 512 + ((rl & ~31) + 8 * ((rl >> 2) & 3) + 4 * ((rl >> 4) & 1) + (rl & 3)) * 2) = (u16)(__float_as_uint(silu_r(bf_lo(cv))) >> 16);
      if (tid < 8) *(LAS v4u*)(L + L_CX + 256 + tid * 16) = v4u{0u, 0u, 0u, 0u};
      smask = __builtin_amdgcn_ballot_w64(tb.y != tagv) == 0 ? tab_decode(tb.x, lane) : tab_wait(rws, tagv, lane, myv);
#pragma unroll
      for (int i = 0; i < 4; ++i) {
        const int xa = (w >> 2) * 64 + (2 * g + (li >> 3)) * 2 + (h >> 4) + 16 * i;
        oa_[i] = PH(OFF_S3 + xa * 16384 + (h & 15) * 1024 + (4 * ch + (w & 3)) * 128 + (li & 7) * 16, myv, smask);
        ol[i] = PH(OFF_S3F + (myv & 3) * 131072 + (xa * 8 + 4 * ch + (w & 3)) * 128 + (h & 7) * 16, myv, smask);
        PIN(oa_[i]); PIN(ol[i]);
      }
      VMWAIT(0);
      BAR();
      const int rr = 4 * g + q4, fr = swz16(rr);
      {
        LAS unsigned char* vb = L + (w < 4 ? L_WV : L_YMW) + rr * 128 + (((2 * (w & 3)) ^ (fq << 1)) + (p4 >> 1)) * 16 + 8 * (p4 & 1);
#pragma unroll
        for (int k = 0; k < 8; ++k) { const v2u lo = trread(vb + (32 * k) * 128), hi = trread(vb + (32 * k + 16) * 128); va[k] = v4u{lo.x, lo.y, hi.x, hi.y}; }
      }
      if (w >= 4 && mr) {
        f4 d = {0.f, 0.f, 0.f, 0.f};
#pragma unroll
        for (int k = 0; k < 7; ++k) d = mfma32(va[k], *(LAS v4u*)(L + L_YMS + (li & 1) * 512 + 64 * k + 16 * g), d);
        if (li < 2) st16(rws, OFF_MR + yi * 512 + li * 256 + (w & 3) * 64 + g * 16, tagged(d, tagv));
      }
#pragma unroll
      for (int t = 0; t < 5; ++t) {
        const int c_ = 2 * (5 * w + t) + (p4 >> 1);
        LAS unsigned char* ob = L + L_WO + rr * 1280 + ((c_ & ~15) | ((c_ & 15) ^ fr)) * 16 + 8 * (p4 & 1);
#pragma unroll
        for (int i = 0; i < 2; ++i) { const v2u lo = trread(ob + i * 40960), hi = trread(ob + i * 40960 + 16 * 1280); oa[i][t] = v4u{lo.x, lo.y, hi.x, hi.y}; }
      }
    }
    int so4 = li < 10 ? PH(OFF_S4 + yi * 5120 + (5 * w + (li >> 1)) * 128 + (g >> 1) * 64 + (li & 1) * 32 + (g & 1) * 16, (5 * w + (li >> 1)) >> 3, smask) : OOB;
    int cxo[5];
#pragma unroll
    for (int t = 0; t < 5; ++t) { cxo[t] = (li >> 1) == t ? L_CX + ((li & 1) * 8 + g) * 16 : L_CX + 256; PIN(cxo[t]); }
    PIN(so4);
    {
      const int b = w >> 2, og = w & 3;
      int cpos = L_CC + (b * 32 + 8 * og + (li & 7)) * 16 + g * 4;
      PIN(cpos);
      gate<true>(rws, (g == 0 ? ol[0] : g == 1 ? ol[1] : g == 2 ? ol[2] : ol[3]) + 4, tagv, lane);
      POLL_BEGIN
      l0 = ld16(rws, ol[0]); l1 = ld16(rws, ol[1]); l2 = ld16(rws, ol[2]); l3 = ld16(rws, ol[3]);
      POLL_END(((ne4(l0, tagv) | ne4(l1, tagv)) | (ne4(l2, tagv) | ne4(l3, tagv))) != 0u)
      asm volatile("" ::: "memory");
      v4u p0 = ld16(rws, oa_[0]), p1 = ld16(rws, oa_[1]), p2 = ld16(rws, oa_[2]), p3 = ld16(rws, oa_[3]);
      __builtin_amdgcn_sched_barrier(0);
      const float f0 = __uint_as_float(l0.x), f1 = __uint_as_float(l1.x), f2_ = __uint_as_float(l2.x), f3 = __uint_as_float(l3.x);
      float m = fmaxf(fmaxf(f0, f1), fmaxf(f2_, f3));
      m = fmaxf(m, ror<8>(m)); m = max32x(max16x(m));
      float w0 = __builtin_amdgcn_exp2f(f0 - m), w1 = __builtin_amdgcn_exp2f(f1 - m), w2 = __builtin_amdgcn_exp2f(f2_ - m), w3 = __builtin_amdgcn_exp2f(f3 - m);
      float S = (w0 + w1) + (w2 + w3);
      S += ror<8>(S); S = sum32x(sum16x(S));
      const float inv = rcp(S);
      w0 *= inv; w1 *= inv; w2 *= inv; w3 *= inv;
      PIN(w0); PIN(w1); PIN(w2); PIN(w3);
#pragma nounroll
      for (u32 sp_ = SPIN_CAP; sp_; --sp_) {
        if (__builtin_amdgcn_ballot_w64(umax(umax(umax4(p0), umax4(p1)), umax(umax4(p2), umax4(p3))) == SENT) == 0) break;
        asm volatile("" ::: "memory");
        p0 = ld16(rws, oa_[0]); p1 = ld16(rws, oa_[1]); p2 = ld16(rws, oa_[2]); p3 = ld16(rws, oa_[3]);
      }
      CWAIT();
      float c[8];
#pragma unroll
      for (int k = 0; k < 4; ++k) {
        c[2 * k] = (w0 * bf_lo(p0[k]) + w1 * bf_lo(p1[k])) + (w2 * bf_lo(p2[k]) + w3 * bf_lo(p3[k]));
        c[2 * k + 1] = (w0 * bf_hi(p0[k]) + w1 * bf_hi(p1[k])) + (w2 * bf_hi(p2[k]) + w3 * bf_hi(p3[k]));
      }
      float t[4], u[2];
#pragma unroll
      for (int i = 0; i < 4; ++i) { float x, y; sw32(c[i], c[i + 4], x, y); t[i] = x + y; }
#pragma unroll
      for (int i = 0; i < 2; ++i) { float x, y; sw16(t[i], t[i + 2], x, y); u[i] = x + y; }
      u[0] += ror<8>(u[0]); u[1] += ror<8>(u[1]);
      if (li < 8) *(LAS u32*)(L + cpos) = pk(u[0], u[1]);
    }
    BAR();
    if (w < 4) {
      f4 a = {0.f, 0.f, 0.f, 0.f};
      v4u cf[8];
#pragma unroll
      for (int k = 0; k < 8; ++k) cf[k] = *(LAS v4u*)(L + L_CC + ((li & 1) * 32 + 4 * k + g) * 16);
      __builtin_amdgcn_sched_barrier(0);
#pragma unroll
      for (int k = 0; k < 8; ++k) a = mfma32(va[k], cf[k], a);
      if (li < 2) *(LAS v2u*)(L + L_CX + (li * 8 + (w >> 1) * 4 + g) * 16 + (w & 1) * 8) = v2u{pk(a[0], a[1]), pk(a[2], a[3])};
    }
    BAR();
    {
      v4u cx[2][5];
#pragma unroll
      for (int t = 0; t < 5; ++t) { cx[0][t] = *(LAS v4u*)(L + cxo[t]); cx[1][t] = *(LAS v4u*)(L + cxo[t] + 64); }
      __builtin_amdgcn_sched_barrier(0);
      f4 o = {0.f, 0.f, 0.f, 0.f};
#pragma unroll
      for (int t = 0; t < 5; ++t) { o = mfma32(oa[0][t], cx[0][t], o); o = mfma32(oa[1][t], cx[1][t], o); }
      st16(rws, so4, tagged(o, tagv));
    }
    if (yi < 80) {
      const int r = yi;
      const int bb = (lane >> 1) & 1, m0r = 8 * r + 4 * (lane & 1);
      f4 ga4 = {0.f, 0.f, 0.f, 0.f}, gb4 = ga4, x4 = ga4;
      if (w == 0) {
        const v2u xv = __builtin_amdgcn_raw_buffer_load_b64(mkrsrc(P_X, 2560), (bb * 640 + m0r) * 2, 0, 0);
        x4 = f4{bf_lo(xv.x), bf_hi(xv.x), bf_lo(xv.y), bf_hi(xv.y)};
        mr_gather(rws, m0r, bb, 0, 3, tagv, ga4, gb4);
#pragma unroll
        for (int i = 0; i < 4; ++i) { ga4[i] = rbf(ga4[i]); gb4[i] = rbf(gb4[i]); }
        PIN(ga4); PIN(gb4); PIN(x4);
      }
      int wpos = ((w * 4 + (lane & 3)) * 4 + ((g & 1) * 2 + (g >> 1))) * 4, o5 = PH(OFF_S5 + (lane >> 3) * 10240 + r * 128 + (lane & 7) * 16, lane >> 3, smask);
      PIN(wpos); PIN(o5);
      if (w) for (int i = 0; i < D4; ++i) __builtin_amdgcn_s_sleep(2);
      v4u a;
      POLL_BEGIN
      a = ld16(rws, PH(OFF_S4 + (tid >> 2) * 5120 + r * 64 + (tid & 3) * 16, myv, smask));
      POLL_END(ne4(a, tagv) != 0u)
      f4 x1 = piece_reduce(unpk(a), L + L_YRED, wpos, lane, w);
      if (w == 0) {
        const v4u xt = tagged(x4 + ga4 * x1, tagv);
        st16(rws, o5, xt);
        x1 = unpk(xt);
      }
      const bool pact = (tid >> 2) < 108;
      for (int i = 0; i < D6; ++i) __builtin_amdgcn_s_sleep(4);
      POLL_BEGIN
      a = ld16(rws, pact ? PH(OFF_S6 + (tid >> 2) * 5120 + r * 64 + (tid & 3) * 16, myv, smask) : OOB);
      POLL_END(pact && ne4(a, tagv) != 0u)
      f4 y = piece_reduce(unpk(a), L + L_YRED2, wpos, lane, w);
      if (w == 0) {
        y = x1 + gb4 * y;
        if (lane < 4) *(GAS v2u*)((GAS u16*)P_OUT + bb * 640 + m0r) = v2u{pk(y[0], y[1]), pk(y[2], y[3])};
        if (r == 0 && lane == 0) st4(rws, OFF_EPOCH, tagv);
      }
      asm volatile(".Lfd_end2:");
    }
  }
  asm volatile(".Lfd_end:");
}

extern "C" void kernel_launch(void* const* d_in, const int* in_sizes, int n_in, void* d_out, int out_size, void* d_ws, size_t ws_size,
                              hipStream_t stream) {
  fused_decode<<<256, 512, 0, stream>>>((const u16*)d_in[0], (const u16*)d_in[1], (const u16*)d_in[2], (const u16*)d_in[3], (const u16*)d_in[4],
                                        (const u16*)d_in[5], (const u16*)d_in[7], (const u16*)d_in[8], (const u16*)d_in[9], (const u16*)d_in[10],
                                        (const u16*)d_in[11], (u16*)d_out, (unsigned char*)d_ws);
}
```
